```python
import torch

def _softmax(x, axis=-1):
    return torch.softmax(x, dim=axis)
def _gelu(x):
    return torch.nn.functional.gelu(x, approximate="tanh")
def _silu(x):
    return torch.nn.functional.silu(x)
def _sigmoid(x):
    return torch.sigmoid(x)
def _layernorm(x, axis=-1, eps=1e-5):
    m = x.mean(dim=axis, keepdim=True)
    v = x.var(dim=axis, keepdim=True, unbiased=False)
    return (x - m) / torch.sqrt(v + eps)
def _scatter_add(tgt, idx, src, axis=0):
    return tgt.clone().index_add_(axis, idx, src)
def _segment_sum(src, seg, n):
    out = torch.zeros((n,) + src.shape[1:], dtype=src.dtype)
    return out.index_add_(0, seg, src)
def _pad(x, pw):
    flat = tuple(v for pair in reversed(pw) for v in pair)
    return torch.nn.functional.pad(x, flat)

kernel_name = 'concat_heads__sliding_block_cached__sliding_block_cached__decode'

def setup_inputs():

    _w = (lambda a: (lambda t: t.to(torch.bfloat16) if t.is_floating_point() else t)(torch.as_tensor(a)))
    def _A_setup():
        B, Sq, D = 8, 1, 512
        Hq, Hkv, Sk, Dh = 32, 8, 4096, 128
        assert Hq % Hkv == 0, (Hq, Hkv)
        return {
            "x": _w((lambda *s: torch.randn(*s, dtype=torch.bfloat16) * 0.8)(B, Sq, D)),
            "k_cache": _w((lambda *s: torch.randn(*s, dtype=torch.bfloat16) * 0.8)(B, Hkv, Sk, Dh)),
            "v_cache": _w((lambda *s: torch.randn(*s, dtype=torch.bfloat16) * 0.05)(B, Hkv, Sk, Dh)),
            "wq": _w((lambda *s: torch.randn(*s, dtype=torch.bfloat16) * 0.8)(D, Hq * Dh) * (D ** -0.5)),
            "wo": _w((lambda *s: torch.randn(*s, dtype=torch.bfloat16) * 0.05)(Hq * Dh, D) * ((Hq * Dh) ** -0.5)),
        }
    _A_in = _A_setup()

    def _B_setup():
        B, Sq, D = 8, 1, 512
        Hq, Hkv, Sk, Dh = 32, 8, 4096, 128
        assert Hq % Hkv == 0, (Hq, Hkv)
        return {
            "x": _w((lambda *s: torch.randn(*s, dtype=torch.bfloat16) * 0.8)(B, Sq, D)),
            "k_cache": _w((lambda *s: torch.randn(*s, dtype=torch.bfloat16) * 0.8)(B, Hkv, Sk, Dh)),
            "v_cache": _w((lambda *s: torch.randn(*s, dtype=torch.bfloat16) * 0.05)(B, Hkv, Sk, Dh)),
            "wq": _w((lambda *s: torch.randn(*s, dtype=torch.bfloat16) * 0.8)(D, Hq * Dh) * (D ** -0.5)),
            "wo": _w((lambda *s: torch.randn(*s, dtype=torch.bfloat16) * 0.05)(Hq * Dh, D) * ((Hq * Dh) ** -0.5)),
        }
    _B_in = _B_setup()

    return {
        "A_x": _A_in["x"],
        "A_k_cache": _A_in["k_cache"],
        "A_v_cache": _A_in["v_cache"],
        "A_wq": _A_in["wq"],
        "A_wo": _A_in["wo"],
        "B_x": _B_in["x"],
        "B_k_cache": _B_in["k_cache"],
        "B_v_cache": _B_in["v_cache"],
        "B_wq": _B_in["wq"],
        "B_wo": _B_in["wo"],
    }

def reference(A_x, A_k_cache, A_v_cache, A_wq, A_wo, B_x, B_k_cache, B_v_cache, B_wq, B_wo):
    def _A_ref(x, k_cache, v_cache, wq, wo):
        B, Sq, D = x.shape
        Hq, Dh = 32, 128
        Hkv, Sk = k_cache.shape[1], k_cache.shape[2]
        W = min(4096, max(256, Sk // 4))
        rep = Hq // Hkv
        q = (lambda x, *d: x.permute(*d))((x @ wq).reshape(B, Sq, Hq, Dh), 0, 2, 1, 3)
        k = (lambda x, n, axis: torch.repeat_interleave(x, n, dim=axis))(k_cache[:, :, Sk - W:, :], rep, axis=1)
        v = (lambda x, n, axis: torch.repeat_interleave(x, n, dim=axis))(v_cache[:, :, Sk - W:, :], rep, axis=1)
        scores = torch.einsum("bhqd,bhkd->bhqk", q, k) * (Dh ** -0.5)
        p = _softmax(scores, axis=-1)
        o = torch.einsum("bhqk,bhkd->bhqd", p, v)
        return (lambda x, *d: x.permute(*d))(o, 0, 2, 1, 3).reshape(B, Sq, Hq * Dh) @ wo

    def _B_ref(x, k_cache, v_cache, wq, wo):
        B, Sq, D = x.shape
        Hq, Dh = 32, 128
        Hkv, Sk = k_cache.shape[1], k_cache.shape[2]
        W = min(4096, max(256, Sk // 4))
        rep = Hq // Hkv
        q = (lambda x, *d: x.permute(*d))((x @ wq).reshape(B, Sq, Hq, Dh), 0, 2, 1, 3)
        k = (lambda x, n, axis: torch.repeat_interleave(x, n, dim=axis))(k_cache[:, :, Sk - W:, :], rep, axis=1)
        v = (lambda x, n, axis: torch.repeat_interleave(x, n, dim=axis))(v_cache[:, :, Sk - W:, :], rep, axis=1)
        scores = torch.einsum("bhqd,bhkd->bhqk", q, k) * (Dh ** -0.5)
        p = _softmax(scores, axis=-1)
        o = torch.einsum("bhqk,bhkd->bhqd", p, v)
        return (lambda x, *d: x.permute(*d))(o, 0, 2, 1, 3).reshape(B, Sq, Hq * Dh) @ wo

    a = _A_ref(A_x, A_k_cache, A_v_cache, A_wq, A_wo)
    b = _B_ref(B_x, B_k_cache, B_v_cache, B_wq, B_wo)
    return torch.concatenate([a, b], axis=-1)
```

```cpp
#include <hip/hip_runtime.h>
#include <hip/hip_bf16.h>
#include <stdint.h>

typedef unsigned v4u __attribute__((ext_vector_type(4)));
typedef unsigned v2u __attribute__((ext_vector_type(2)));
typedef float f32x4 __attribute__((ext_vector_type(4)));
typedef float f32x2 __attribute__((ext_vector_type(2)));
typedef __bf16 bf16x8 __attribute__((ext_vector_type(8)));
typedef short s16x4 __attribute__((ext_vector_type(4)));
typedef unsigned long long u64;

#define LAS __attribute__((address_space(3)))

#define N1 12
#define N2 8
#define NW 8
#define AUX_STREAM 2
#define STAMPS 0

constexpr int AUX_SC1 = 16;
constexpr unsigned SPIN_CAP = 1u << 16;

constexpr int WS_EPOCH = 0;
constexpr int WS_QG = 4096;
constexpr int WS_OG = WS_QG + 2 * 8 * 8 * 512 * 8;
constexpr int WS_LG = WS_OG + 2 * 8 * 4 * 16 * 128 * 8;
constexpr int WS_PG = WS_LG + 2 * 8 * 4 * 16 * 8;
constexpr int WS_END = WS_PG + 2 * 4 * 32 * 32 * 32 * 8;
constexpr int WS_STAMP = 8 << 20;

struct Params {
  const void* x[2];
  const void* k[2];
  const void* v[2];
  const void* wq[2];
  const void* wo[2];
  void* out;
  void* ws;
};

__device__ __forceinline__ __amdgpu_buffer_rsrc_t mk_rsrc(const void* p, unsigned bytes) {
  return __builtin_amdgcn_make_buffer_rsrc(const_cast<void*>(p), 0, bytes, 0x00020000);
}
__device__ __forceinline__ f32x4 mfma32(v4u a, v4u b, f32x4 c) {
  return __builtin_amdgcn_mfma_f32_16x16x32_bf16(__builtin_bit_cast(bf16x8, a), __builtin_bit_cast(bf16x8, b), c, 0, 0, 0);
}
__device__ __forceinline__ f32x4 mfma16(v2u a, v2u b, f32x4 c) {
  return __builtin_amdgcn_mfma_f32_16x16x16bf16_1k(__builtin_bit_cast(s16x4, a), __builtin_bit_cast(s16x4, b), c, 0, 0, 0);
}
__device__ __forceinline__ unsigned perm_lo(unsigned a, unsigned b) { return __builtin_amdgcn_perm(b, a, 0x05040100u); }
__device__ __forceinline__ unsigned perm_hi(unsigned a, unsigned b) { return __builtin_amdgcn_perm(b, a, 0x07060302u); }
__device__ __forceinline__ unsigned cvt_pk_bf16(float lo, float hi) {
  typedef __bf16 bf16x2 __attribute__((ext_vector_type(2)));
  bf16x2 r = {(__bf16)lo, (__bf16)hi};
  return __builtin_bit_cast(unsigned, r);
}
template <int CTRL> __device__ __forceinline__ float dpp(float x) {
  return __builtin_bit_cast(float, __builtin_amdgcn_mov_dpp(__builtin_bit_cast(int, x), CTRL, 0xf, 0xf, true));
}
__device__ __forceinline__ float xrow_max(float x) {
  auto s = __builtin_amdgcn_permlane16_swap(__float_as_uint(x), __float_as_uint(x), false, false);
  x = fmaxf(__uint_as_float(s[0]), __uint_as_float(s[1]));
  auto t = __builtin_amdgcn_permlane32_swap(__float_as_uint(x), __float_as_uint(x), false, false);
  return fmaxf(__uint_as_float(t[0]), __uint_as_float(t[1]));
}
__device__ __forceinline__ float xrow_sum(float x) {
  auto s = __builtin_amdgcn_permlane16_swap(__float_as_uint(x), __float_as_uint(x), false, false);
  x = __uint_as_float(s[0]) + __uint_as_float(s[1]);
  auto t = __builtin_amdgcn_permlane32_swap(__float_as_uint(x), __float_as_uint(x), false, false);
  return __uint_as_float(t[0]) + __uint_as_float(t[1]);
}
#define WG_BARRIER() asm volatile("s_waitcnt lgkmcnt(0)\n\ts_barrier" ::: "memory")
#define SCHED_FENCE() __builtin_amdgcn_sched_barrier(0)

__device__ __forceinline__ unsigned f32_to_f24(float f) {
  unsigned u = __float_as_uint(f);
  unsigned r = (u + 0x7Fu + ((u >> 8) & 1u)) >> 8;
  return ((u & 0x7F800000u) == 0x7F800000u) ? ((u >> 8) | ((u & 0xFFu) ? 1u : 0u)) : r;
}

#define STAMP(i) do {} while (0)

__global__ void __launch_bounds__(512, 2) fused_decode(Params p) {
  __shared__ __attribute__((aligned(16))) float lds[8 * 8 * 128 + 1024];
  float* red = lds;
  unsigned short* qs = (unsigned short*)(lds + 8 * 8 * 128);
  float* mx = lds + 8 * 8 * 128 + 256;
  float* ls = mx + 32;

  const int tid = threadIdx.x, lane = tid & 63, w = __builtin_amdgcn_readfirstlane(tid >> 6);
  const int bid = blockIdx.x, kvh = bid & 7, br = bid >> 7, mem = (bid >> 3) & 15;
  const int g = lane >> 4, c = lane & 15;
  const int b = mem >> 1, half = mem & 1, hq = mem >> 2, cq = mem & 3;
  STAMP(0);

  const __amdgpu_buffer_rsrc_t rws = mk_rsrc(p.ws, WS_END);
  const __amdgpu_buffer_rsrc_t rx = mk_rsrc(br ? p.x[1] : p.x[0], 8 * 512 * 2);
  const __amdgpu_buffer_rsrc_t rwq = mk_rsrc(br ? p.wq[1] : p.wq[0], 512 * 4096 * 2);
  const __amdgpu_buffer_rsrc_t rwo = mk_rsrc(br ? p.wo[1] : p.wo[0], 512 * 4096 * 2);
  const size_t kvbase = ((size_t)((b * 8 + kvh) * 4096 + 3072 + 512 * half + 64 * w)) * 256;
  const __amdgpu_buffer_rsrc_t rk = mk_rsrc((const char*)(br ? p.k[1] : p.k[0]) + kvbase, 64 * 256);
  const __amdgpu_buffer_rsrc_t rv = mk_rsrc((const char*)(br ? p.v[1] : p.v[0]) + kvbase, 64 * 256);

  const v2u ep = __builtin_amdgcn_raw_buffer_load_b64(rws, WS_EPOCH + br * 256, 0, AUX_SC1);
  v4u xa[2];
#pragma unroll
  for (int s = 0; s < 2; ++s) xa[s] = __builtin_amdgcn_raw_buffer_load_b128(rx, ((c & 7) * 512 + 64 * w + 32 * s + 8 * g) * 2, 0, 0);
  unsigned wqr[16];
  {
    const int off = ((64 * w + 8 * g) * 4096 + kvh * 512 + 32 * mem + 2 * c) * 2;
#pragma unroll
    for (int i = 0; i < 16; ++i) wqr[i] = __builtin_amdgcn_raw_buffer_load_b32(rwq, off, ((i >> 3) * 32 + (i & 7)) * 8192, AUX_STREAM);
  }
  SCHED_FENCE();
  v4u K[16], V[16];
  const int koff = 256 * ((lane >> 2) & 3) + 64 * g + 16 * (lane & 3);
#pragma unroll
  for (int i = 0; i < N1; ++i) K[i] = __builtin_amdgcn_raw_buffer_load_b128(rk, koff + (i & 3) * 1024, (i >> 2) * 4096, AUX_STREAM);
  SCHED_FENCE();

  {
    f32x4 acc0 = {0.f, 0.f, 0.f, 0.f}, acc1 = {0.f, 0.f, 0.f, 0.f};
#pragma unroll
    for (int s = 0; s < 2; ++s) {
      v4u b0, b1;
#pragma unroll
      for (int t = 0; t < 4; ++t) { b0[t] = perm_lo(wqr[8 * s + 2 * t], wqr[8 * s + 2 * t + 1]); b1[t] = perm_hi(wqr[8 * s + 2 * t], wqr[8 * s + 2 * t + 1]); }
      acc0 = mfma32(xa[s], b0, acc0);
      acc1 = mfma32(xa[s], b1, acc1);
    }
    if (g < 2) {
#pragma unroll
      for (int r = 0; r < 4; ++r) *(f32x2*)&red[(w * 8 + 4 * g + r) * 32 + 2 * c] = f32x2{acc0[r], acc1[r]};
    }
  }
  const u64 T = (((u64)__builtin_amdgcn_readfirstlane(ep[1]) << 32) | (u64)__builtin_amdgcn_readfirstlane(ep[0])) + 1ull;
  const unsigned t48_lo = (unsigned)(T << 16), t48_hi = (unsigned)(T >> 16);
  const unsigned t40_lo = (unsigned)(T << 24), t40_hi = (unsigned)(T >> 8);
  STAMP(1);
  WG_BARRIER();
  if (tid < 256) {
    float s = 0.f;
#pragma unroll
    for (int ww = 0; ww < 8; ++ww) s += red[ww * 256 + tid];
    const unsigned qb = cvt_pk_bf16(s, 0.f) & 0xFFFFu;
    const int bb = tid >> 5, col = tid & 31;
    __builtin_amdgcn_raw_buffer_store_b64(v2u{qb | t48_lo, t48_hi}, rws, WS_QG + ((((br * 8 + kvh) * 8 + bb) * 512) + 32 * mem + col) * 8, 0, AUX_SC1);
  }
  STAMP(2);
  SCHED_FENCE();
#pragma unroll
  for (int i = N1; i < 16; ++i) K[i] = __builtin_amdgcn_raw_buffer_load_b128(rk, koff + (i & 3) * 1024, (i >> 2) * 4096, AUX_STREAM);
#pragma unroll
  for (int i = 0; i < N2; ++i) V[i] = __builtin_amdgcn_raw_buffer_load_b128(rv, lane * 16 + (i & 3) * 1024, (i >> 2) * 4096, AUX_STREAM);
  SCHED_FENCE();
  asm volatile("" : "+v"(K[NW - 1]));
  SCHED_FENCE();
  STAMP(3);
  const int qoff = WS_QG + (((br * 8 + kvh) * 8 + b) * 512 + tid) * 8;
  v2u qg = __builtin_amdgcn_raw_buffer_load_b64(rws, qoff, 0, AUX_SC1);
  SCHED_FENCE();
#pragma unroll
  for (int i = N2; i < 16; ++i) V[i] = __builtin_amdgcn_raw_buffer_load_b128(rv, lane * 16 + (i & 3) * 1024, (i >> 2) * 4096, AUX_STREAM);
  v4u wo[4];
  {
    const int off = (((kvh * 4 + hq) * 128 + 16 * w + 4 * g) * 512 + 128 * cq + 8 * c) * 2;
#pragma unroll
    for (int i = 0; i < 4; ++i) wo[i] = __builtin_amdgcn_raw_buffer_load_b128(rwo, off + i * 1024, 0, AUX_STREAM);
  }
  SCHED_FENCE();
  {
    unsigned spins = 0;
    while (!__all(((qg[0] & 0xFFFF0000u) == t48_lo) && (qg[1] == t48_hi))) {
      if (++spins > SPIN_CAP) break;
      __builtin_amdgcn_s_sleep(1);
      asm volatile("" ::: "memory");
      qg = __builtin_amdgcn_raw_buffer_load_b64(rws, qoff, 0, AUX_SC1);
    }
  }
  qs[tid] = (unsigned short)qg[0];
  STAMP(4);
  WG_BARRIER();
  const int hl = (lane >> 2) & 3;
  const v4u qf = *(const v4u*)(qs + 128 * hl + 32 * g + 8 * (lane & 3));
  float sc[16];
#pragma unroll
  for (int i = 0; i < 16; ++i) {
    const f32x4 d = mfma32(K[i], qf, f32x4{0.f, 0.f, 0.f, 0.f});
    sc[i] = (dpp<0x00>(d[0]) + dpp<0x55>(d[1])) + (dpp<0xAA>(d[2]) + dpp<0xFF>(d[3]));
  }
  float m = sc[0];
#pragma unroll
  for (int i = 1; i < 16; ++i) m = fmaxf(m, sc[i]);
  m = xrow_max(m);
  if (lane < 16 && (lane & 3) == 0) mx[w * 4 + hl] = m;
  WG_BARRIER();
  float M = mx[hl];
#pragma unroll
  for (int ww = 1; ww < 8; ++ww) M = fmaxf(M, mx[ww * 4 + hl]);
  constexpr float C2 = 0.088388347648318440f * 1.4426950408889634f;
  const float Mc = M * C2;
  float l = 0.f;
#pragma unroll
  for (int i = 0; i < 16; ++i) { sc[i] = __builtin_amdgcn_exp2f(fmaf(sc[i], C2, -Mc)); l += sc[i]; }
  l = xrow_sum(l);
  if (lane < 16 && (lane & 3) == 0) ls[w * 4 + hl] = l;
  v4u pa[2];
#pragma unroll
  for (int u = 0; u < 2; ++u)
#pragma unroll
    for (int t = 0; t < 4; ++t) pa[u][t] = cvt_pk_bf16(sc[8 * u + 2 * t], sc[8 * u + 2 * t + 1]);
  STAMP(5);
  f32x4 oa[8];
#pragma unroll
  for (int dd = 0; dd < 8; ++dd) oa[dd] = f32x4{0.f, 0.f, 0.f, 0.f};
#pragma unroll
  for (int u = 0; u < 2; ++u) {
#pragma unroll
    for (int dd = 0; dd < 8; ++dd) {
      v4u bf;
#pragma unroll
      for (int t = 0; t < 4; ++t)
        bf[t] = (dd & 1) ? perm_hi(V[8 * u + 2 * t][dd >> 1], V[8 * u + 2 * t + 1][dd >> 1]) : perm_lo(V[8 * u + 2 * t][dd >> 1], V[8 * u + 2 * t + 1][dd >> 1]);
      oa[dd] = mfma32(pa[u], bf, oa[dd]);
    }
  }
  *(f32x4*)&red[(w * 4 + g) * 128 + 8 * c] = f32x4{oa[0][0], oa[1][0], oa[2][0], oa[3][0]};
  *(f32x4*)&red[(w * 4 + g) * 128 + 8 * c + 4] = f32x4{oa[4][0], oa[5][0], oa[6][0], oa[7][0]};
  STAMP(6);
  WG_BARRIER();
  {
    const int h = tid >> 7;
    float s = 0.f, L = 0.f, Mh = mx[h];
#pragma unroll
    for (int ww = 0; ww < 8; ++ww) { s += red[ww * 512 + tid]; L += ls[ww * 4 + h]; Mh = fmaxf(Mh, mx[ww * 4 + h]); }
    const unsigned ob = cvt_pk_bf16(s / L, 0.f) & 0xFFFFu;
    const int rowi = ((br * 8 + kvh) * 4 + h) * 16 + mem;
    __builtin_amdgcn_raw_buffer_store_b64(v2u{ob | t48_lo, t48_hi}, rws, WS_OG + (rowi * 128 + (tid & 127)) * 8, 0, AUX_SC1);
    if ((tid & 127) == 0) {
      const float lse = fmaf(Mh, C2, __builtin_amdgcn_logf(L));
      __builtin_amdgcn_raw_buffer_store_b64(v2u{f32_to_f24(lse) | t40_lo, t40_hi}, rws, WS_LG + rowi * 8, 0, AUX_SC1);
    }
  }
  STAMP(7);
  v2u wb[8];
#pragma unroll
  for (int dd = 0; dd < 8; ++dd) {
    wb[dd][0] = (dd & 1) ? perm_hi(wo[0][dd >> 1], wo[1][dd >> 1]) : perm_lo(wo[0][dd >> 1], wo[1][dd >> 1]);
    wb[dd][1] = (dd & 1) ? perm_hi(wo[2][dd >> 1], wo[3][dd >> 1]) : perm_lo(wo[2][dd >> 1], wo[3][dd >> 1]);
  }
  {
    const int rows = ((br * 8 + kvh) * 4 + hq) * 16;
    const int ooff = WS_OG + ((rows + c) * 128 + 16 * w + 4 * g) * 8;
    const int loff = WS_LG + (rows + 4 * g) * 8;
    v4u o0, o1, l0, l1;
    unsigned spins = 0;
    for (;;) {
      asm volatile("" ::: "memory");
      o0 = __builtin_amdgcn_raw_buffer_load_b128(rws, ooff, 0, AUX_SC1);
      o1 = __builtin_amdgcn_raw_buffer_load_b128(rws, ooff + 16, 0, AUX_SC1);
      l0 = __builtin_amdgcn_raw_buffer_load_b128(rws, loff, 0, AUX_SC1);
      l1 = __builtin_amdgcn_raw_buffer_load_b128(rws, loff + 16, 0, AUX_SC1);
      const unsigned bad = ((o0[0] & 0xFFFF0000u) ^ t48_lo) | (o0[1] ^ t48_hi) | ((o0[2] & 0xFFFF0000u) ^ t48_lo) | (o0[3] ^ t48_hi) |
                           ((o1[0] & 0xFFFF0000u) ^ t48_lo) | (o1[1] ^ t48_hi) | ((o1[2] & 0xFFFF0000u) ^ t48_lo) | (o1[3] ^ t48_hi) |
                           ((l0[0] & 0xFF000000u) ^ t40_lo) | (l0[1] ^ t40_hi) | ((l0[2] & 0xFF000000u) ^ t40_lo) | (l0[3] ^ t40_hi) |
                           ((l1[0] & 0xFF000000u) ^ t40_lo) | (l1[1] ^ t40_hi) | ((l1[2] & 0xFF000000u) ^ t40_lo) | (l1[3] ^ t40_hi);
      if (__all(bad == 0u)) break;
      if (++spins > SPIN_CAP) break;
      __builtin_amdgcn_s_sleep(1);
    }
    STAMP(8);
    const v2u af = {perm_lo(o0[0], o0[2]), perm_lo(o1[0], o1[2])};
    const float e0 = __uint_as_float(l0[0] << 8), e1 = __uint_as_float(l0[2] << 8), e2 = __uint_as_float(l1[0] << 8), e3 = __uint_as_float(l1[2] << 8);
    const float ma = fmaxf(e0, e1), mb = fmaxf(e2, e3);
    const float x0 = __builtin_amdgcn_exp2f(e0 - ma), x1 = __builtin_amdgcn_exp2f(e1 - ma), x2 = __builtin_amdgcn_exp2f(e2 - mb), x3 = __builtin_amdgcn_exp2f(e3 - mb);
    const float ra = 1.f / (x0 + x1), rb = 1.f / (x2 + x3);
    const float a0 = x0 * ra, a1 = x1 * ra, a2 = x2 * rb, a3 = x3 * rb;
    float y0[8], y1[8];
#pragma unroll
    for (int dd = 0; dd < 8; ++dd) {
      const f32x4 d = mfma16(af, wb[dd], f32x4{0.f, 0.f, 0.f, 0.f});
      y0[dd] = a0 * d[0] + a1 * d[1];
      y1[dd] = a2 * d[2] + a3 * d[3];
    }
    WG_BARRIER();
    float* r0 = &red[(w * 8 + 2 * g) * 128 + 8 * c];
    *(f32x4*)(r0) = f32x4{y0[0], y0[1], y0[2], y0[3]};
    *(f32x4*)(r0 + 4) = f32x4{y0[4], y0[5], y0[6], y0[7]};
    *(f32x4*)(r0 + 128) = f32x4{y1[0], y1[1], y1[2], y1[3]};
    *(f32x4*)(r0 + 132) = f32x4{y1[4], y1[5], y1[6], y1[7]};
  }
  WG_BARRIER();
  {
    f32x2 s = {0.f, 0.f};
#pragma unroll
    for (int ww = 0; ww < 8; ++ww) s += *(const f32x2*)&red[ww * 1024 + 2 * tid];
    const int bb = w, col = 2 * lane;
    const int cons = 4 * bb + (col >> 5), prod = kvh * 4 + hq;
    __builtin_amdgcn_raw_buffer_store_b128(v4u{f32_to_f24(s[0]) | t40_lo, t40_hi, f32_to_f24(s[1]) | t40_lo, t40_hi}, rws,
                                           WS_PG + ((((br * 4 + cq) * 32 + cons) * 32 + prod) * 32 + (col & 31)) * 8, 0, AUX_SC1);
  }
  STAMP(9);
  if (w == 0) {
    const int jj = kvh * 4 + hq;
    const int poff = WS_PG + ((((br * 4 + cq) * 32 + jj) * 32 + g) * 32 + 2 * c) * 8;
    v4u pg[8];
    unsigned spins = 0;
    for (;;) {
      asm volatile("" ::: "memory");
      unsigned bad = 0;
#pragma unroll
      for (int i = 0; i < 8; ++i) pg[i] = __builtin_amdgcn_raw_buffer_load_b128(rws, poff + (i & 3) * 1024, (i >> 2) * 4096, AUX_SC1);
#pragma unroll
      for (int i = 0; i < 8; ++i)
        bad |= ((pg[i][0] & 0xFF000000u) ^ t40_lo) | (pg[i][1] ^ t40_hi) | ((pg[i][2] & 0xFF000000u) ^ t40_lo) | (pg[i][3] ^ t40_hi);
      if (__all(bad == 0u)) break;
      if (++spins > SPIN_CAP) break;
      __builtin_amdgcn_s_sleep(1);
    }
    float s0 = 0.f, s1 = 0.f;
#pragma unroll
    for (int i = 0; i < 8; ++i) { s0 += __uint_as_float(pg[i][0] << 8); s1 += __uint_as_float(pg[i][2] << 8); }
    s0 = xrow_sum(s0);
    s1 = xrow_sum(s1);
    if (lane < 16) {
      const int bb = jj >> 2, sub = jj & 3;
      ((unsigned*)p.out)[(bb * 1024 + br * 512 + 128 * cq + 32 * sub) / 2 + c] = cvt_pk_bf16(s0, s1);
    }
    if (jj == 0 && cq == 0 && lane == 0)
      __builtin_amdgcn_raw_buffer_store_b64(v2u{(unsigned)T, (unsigned)(T >> 32)}, rws, WS_EPOCH + br * 256, 0, AUX_SC1);
    STAMP(10);
  }
}

extern "C" void kernel_launch(void* const* d_in, const int* in_sizes, int n_in, void* d_out, int out_size, void* d_ws, size_t ws_size,
                              hipStream_t stream) {
  Params p;
  p.x[0] = d_in[0]; p.k[0] = d_in[1]; p.v[0] = d_in[2]; p.wq[0] = d_in[3]; p.wo[0] = d_in[4];
  p.x[1] = d_in[5]; p.k[1] = d_in[6]; p.v[1] = d_in[7]; p.wq[1] = d_in[8]; p.wo[1] = d_in[9];
  p.out = d_out; p.ws = d_ws;
  hipLaunchKernelGGL(fused_decode, dim3(256), dim3(512), 0, stream, p);
}
```

```cpp
#include <hip/hip_runtime.h>
#include <hip/hip_bf16.h>
#include <stdint.h>

typedef unsigned v4u __attribute__((ext_vector_type(4)));
typedef unsigned v2u __attribute__((ext_vector_type(2)));
typedef float f32x4 __attribute__((ext_vector_type(4)));
typedef float f32x2 __attribute__((ext_vector_type(2)));
typedef __bf16 bf16x8 __attribute__((ext_vector_type(8)));
typedef short s16x4 __attribute__((ext_vector_type(4)));
typedef unsigned long long u64;

#define N1 4
#define NB 0
#define AUX_STREAM 2
#define STAMPS 0

constexpr int AUX_SC1 = 16;
constexpr unsigned SPIN_CAP = 1u << 16;

constexpr int WS_EPOCH = 0;
constexpr int WS_QG = 4096;
constexpr int WS_OG = WS_QG + 2 * 8 * 8 * 2048;
constexpr int WS_LG = WS_OG + 2 * 8 * 4 * 8 * 1024;
constexpr int WS_PG = WS_LG + 2 * 8 * 4 * 8 * 32;
constexpr int WS_END = WS_PG + 2 * 4 * 32 * 32 * 256;
constexpr int WS_STAMP = 8 << 20;

struct Params {
  const void* x[2];
  const void* k[2];
  const void* v[2];
  const void* wq[2];
  const void* wo[2];
  void* out;
  void* ws;
};

__device__ __forceinline__ __amdgpu_buffer_rsrc_t mk_rsrc(const void* p, unsigned bytes) {
  return __builtin_amdgcn_make_buffer_rsrc(const_cast<void*>(p), 0, bytes, 0x00020000);
}
__device__ __forceinline__ f32x4 mfma32(v4u a, v4u b, f32x4 c) {
  return __builtin_amdgcn_mfma_f32_16x16x32_bf16(__builtin_bit_cast(bf16x8, a), __builtin_bit_cast(bf16x8, b), c, 0, 0, 0);
}
__device__ __forceinline__ f32x4 mfma16(v2u a, v2u b, f32x4 c) {
  return __builtin_amdgcn_mfma_f32_16x16x16bf16_1k(__builtin_bit_cast(s16x4, a), __builtin_bit_cast(s16x4, b), c, 0, 0, 0);
}
__device__ __forceinline__ unsigned perm_lo(unsigned a, unsigned b) { return __builtin_amdgcn_perm(b, a, 0x05040100u); }
__device__ __forceinline__ unsigned perm_hi(unsigned a, unsigned b) { return __builtin_amdgcn_perm(b, a, 0x07060302u); }
__device__ __forceinline__ unsigned cvt_pk_bf16(float lo, float hi) {
  typedef __bf16 bf16x2 __attribute__((ext_vector_type(2)));
  bf16x2 r = {(__bf16)lo, (__bf16)hi};
  return __builtin_bit_cast(unsigned, r);
}
template <int CTRL> __device__ __forceinline__ float dpp(float x) {
  return __builtin_bit_cast(float, __builtin_amdgcn_mov_dpp(__builtin_bit_cast(int, x), CTRL, 0xf, 0xf, true));
}
__device__ __forceinline__ float xrow_max(float x) {
  auto s = __builtin_amdgcn_permlane16_swap(__float_as_uint(x), __float_as_uint(x), false, false);
  x = fmaxf(__uint_as_float(s[0]), __uint_as_float(s[1]));
  auto t = __builtin_amdgcn_permlane32_swap(__float_as_uint(x), __float_as_uint(x), false, false);
  return fmaxf(__uint_as_float(t[0]), __uint_as_float(t[1]));
}
__device__ __forceinline__ float xrow_sum(float x) {
  auto s = __builtin_amdgcn_permlane16_swap(__float_as_uint(x), __float_as_uint(x), false, false);
  x = __uint_as_float(s[0]) + __uint_as_float(s[1]);
  auto t = __builtin_amdgcn_permlane32_swap(__float_as_uint(x), __float_as_uint(x), false, false);
  return __uint_as_float(t[0]) + __uint_as_float(t[1]);
}
#define WG_BARRIER() asm volatile("s_waitcnt lgkmcnt(0)\n\ts_barrier" ::: "memory")
#define SCHED_FENCE() __builtin_amdgcn_sched_barrier(0)

#define STAMP(i) do {} while (0)
#define STAMPV(i, v) do {} while (0)
#define STAMP_DUMP() do {} while (0)

__global__ void __launch_bounds__(512, 2) fused_decode(Params p) {
  __shared__ __attribute__((aligned(16))) float lds[8 * 8 * 128 + 8 * 72 + 64];
  float* red = lds;
  unsigned* oa_s = (unsigned*)(lds + 8 * 8 * 128);
  float* mx = lds + 8 * 8 * 128 + 8 * 72;
  float* ls = mx + 32;

  const int tid = threadIdx.x, lane = tid & 63, w = __builtin_amdgcn_readfirstlane(tid >> 6);
  const int bid = blockIdx.x, kvh = bid & 7, br = bid >> 7, mem = (bid >> 3) & 15;
  const int g = lane >> 4, c = lane & 15;
  const int b = mem >> 1, half = mem & 1, hq = mem >> 2, cq = mem & 3;
  const int grp = br * 8 + kvh;
  STAMP(0);

  const __amdgpu_buffer_rsrc_t rws = mk_rsrc(p.ws, WS_END);
  const __amdgpu_buffer_rsrc_t rx = mk_rsrc(br ? p.x[1] : p.x[0], 8 * 512 * 2);
  const __amdgpu_buffer_rsrc_t rwq = mk_rsrc(br ? p.wq[1] : p.wq[0], 512 * 4096 * 2);
  const __amdgpu_buffer_rsrc_t rwo = mk_rsrc(br ? p.wo[1] : p.wo[0], 512 * 4096 * 2);
  const size_t kvbase = ((size_t)((b * 8 + kvh) * 4096 + 3072 + 512 * half + 64 * w)) * 256;
  const __amdgpu_buffer_rsrc_t rk = mk_rsrc((const char*)(br ? p.k[1] : p.k[0]) + kvbase, 64 * 256);
  const __amdgpu_buffer_rsrc_t rv = mk_rsrc((const char*)(br ? p.v[1] : p.v[0]) + kvbase, 64 * 256);

  unsigned wqr[16];
  {
    const int off = ((64 * w + 8 * g) * 4096 + kvh * 512 + 32 * mem + 2 * c) * 2;
#pragma unroll
    for (int i = 0; i < 16; ++i) wqr[i] = __builtin_amdgcn_raw_buffer_load_b32(rwq, off, ((i >> 3) * 32 + (i & 7)) * 8192, AUX_STREAM);
  }
  v4u xa[2];
#pragma unroll
  for (int s = 0; s < 2; ++s) xa[s] = __builtin_amdgcn_raw_buffer_load_b128(rx, ((c & 7) * 512 + 64 * w + 32 * s + 8 * g) * 2, 0, 0);
  const v2u ep = __builtin_amdgcn_raw_buffer_load_b64(rws, WS_EPOCH + br * 256, 0, AUX_SC1);
  SCHED_FENCE();
  v4u K[16], V[16];
  const int koff = 256 * ((lane >> 2) & 3) + 64 * g + 16 * (lane & 3);
#pragma unroll
  for (int i = 0; i < N1; ++i) K[i] = __builtin_amdgcn_raw_buffer_load_b128(rk, koff + (i & 3) * 1024, (i >> 2) * 4096, AUX_STREAM);
  SCHED_FENCE();

  {
    f32x4 acc0 = {0.f, 0.f, 0.f, 0.f}, acc1 = {0.f, 0.f, 0.f, 0.f};
#pragma unroll
    for (int s = 0; s < 2; ++s) {
      v4u b0, b1;
#pragma unroll
      for (int t = 0; t < 4; ++t) { b0[t] = perm_lo(wqr[8 * s + 2 * t], wqr[8 * s + 2 * t + 1]); b1[t] = perm_hi(wqr[8 * s + 2 * t], wqr[8 * s + 2 * t + 1]); }
      acc0 = mfma32(xa[s], b0, acc0);
      acc1 = mfma32(xa[s], b1, acc1);
    }
    if (g < 2) {
#pragma unroll
      for (int r = 0; r < 4; ++r) *(f32x2*)&red[(w * 8 + 4 * g + r) * 32 + 2 * c] = f32x2{acc0[r], acc1[r]};
    }
  }
  const u64 T = (((u64)__builtin_amdgcn_readfirstlane(ep[1]) << 32) | (u64)__builtin_amdgcn_readfirstlane(ep[0])) + 1ull;
  const unsigned tagA = (unsigned)T, tagB = (unsigned)(T >> 32) ^ (unsigned)T;
  STAMP(1);
  WG_BARRIER();
  if (w == 0) {
    __builtin_amdgcn_s_setprio(3);
    f32x4 s = *(const f32x4*)&red[4 * lane];
#pragma unroll
    for (int ww = 1; ww < 8; ++ww) s += *(const f32x4*)&red[ww * 256 + 4 * lane];
    const int bb = lane >> 3, t = (lane >> 1) & 3, i = lane & 1;
    __builtin_amdgcn_raw_buffer_store_b128(v4u{cvt_pk_bf16(s[0], s[1]), tagA, cvt_pk_bf16(s[2], s[3]), tagB}, rws,
                                           WS_QG + (grp * 8 + bb) * 2048 + i * 1024 + (16 * cq + 4 * hq + t) * 16, 0, AUX_SC1);
    SCHED_FENCE();
    __builtin_amdgcn_s_setprio(0);
  }
  STAMP(2);
  SCHED_FENCE();
#pragma unroll
  for (int i = N1; i < 16; ++i) K[i] = __builtin_amdgcn_raw_buffer_load_b128(rk, koff + (i & 3) * 1024, (i >> 2) * 4096, AUX_STREAM);
#pragma unroll
  for (int i = 0; i < NB; ++i) V[i] = __builtin_amdgcn_raw_buffer_load_b128(rv, lane * 16 + (i & 3) * 1024, (i >> 2) * 4096, AUX_STREAM);
  SCHED_FENCE();
  asm volatile("s_barrier" ::: "memory");
  SCHED_FENCE();
  STAMP(3);
  const int qoff = WS_QG + (grp * 8 + b) * 2048 + lane * 16;
  v4u q0 = __builtin_amdgcn_raw_buffer_load_b128(rws, qoff, 0, AUX_SC1);
  v4u q1 = __builtin_amdgcn_raw_buffer_load_b128(rws, qoff + 1024, 0, AUX_SC1);
  SCHED_FENCE();
#pragma unroll
  for (int i = NB; i < 16; ++i) V[i] = __builtin_amdgcn_raw_buffer_load_b128(rv, lane * 16 + (i & 3) * 1024, (i >> 2) * 4096, AUX_STREAM);
  SCHED_FENCE();
  STAMP(11);
  {
    unsigned spins = 0;
    if (__builtin_expect(!__all(((q0[1] ^ tagA) | (q0[3] ^ tagB) | (q1[1] ^ tagA) | (q1[3] ^ tagB)) == 0u), 0)) {
      bool ok;
      do {
        asm volatile("; q re-poll" ::: "memory");
        q0 = __builtin_amdgcn_raw_buffer_load_b128(rws, qoff, 0, AUX_SC1);
        q1 = __builtin_amdgcn_raw_buffer_load_b128(rws, qoff + 1024, 0, AUX_SC1);
        ok = __all(((q0[1] ^ tagA) | (q0[3] ^ tagB) | (q1[1] ^ tagA) | (q1[3] ^ tagB)) == 0u);
        __builtin_amdgcn_s_sleep(1);
      } while (!ok && ++spins < SPIN_CAP);
      ++spins;
    }
    STAMPV(12, spins);
  }
  STAMP(4);
  const int hl = (lane >> 2) & 3;
  const v4u qf = {q0[0], q0[2], q1[0], q1[2]};
  float sc[16];
#pragma unroll
  for (int i = 0; i < 16; ++i) {
    const f32x4 d = mfma32(K[i], qf, f32x4{0.f, 0.f, 0.f, 0.f});
    sc[i] = (dpp<0x00>(d[0]) + dpp<0x55>(d[1])) + (dpp<0xAA>(d[2]) + dpp<0xFF>(d[3]));
  }
  float m = sc[0];
#pragma unroll
  for (int i = 1; i < 16; ++i) m = fmaxf(m, sc[i]);
  m = xrow_max(m);
  if (lane < 16 && (lane & 3) == 0) mx[hl * 8 + w] = m;
  WG_BARRIER();
  SCHED_FENCE();
  v4u wo[4];
  {
    const int off = (((kvh * 4 + hq) * 128 + 16 * w + 4 * g) * 512 + 128 * cq + 8 * c) * 2;
#pragma unroll
    for (int i = 0; i < 4; ++i) wo[i] = __builtin_amdgcn_raw_buffer_load_b128(rwo, off + i * 1024, 0, AUX_STREAM);
  }
  SCHED_FENCE();
  constexpr float C2 = 0.088388347648318440f * 1.4426950408889634f;
  float Mc;
  {
    const f32x4 m0 = *(const f32x4*)&mx[hl * 8], m1 = *(const f32x4*)&mx[hl * 8 + 4];
    Mc = fmaxf(fmaxf(fmaxf(m0[0], m0[1]), fmaxf(m0[2], m0[3])), fmaxf(fmaxf(m1[0], m1[1]), fmaxf(m1[2], m1[3]))) * C2;
  }
  float l = 0.f;
#pragma unroll
  for (int i = 0; i < 16; ++i) { sc[i] = __builtin_amdgcn_exp2f(fmaf(sc[i], C2, -Mc)); l += sc[i]; }
  l = xrow_sum(l);
  if (lane < 16 && (lane & 3) == 0) ls[hl * 8 + w] = l;
  v4u pa[2];
#pragma unroll
  for (int u = 0; u < 2; ++u)
#pragma unroll
    for (int t = 0; t < 4; ++t) pa[u][t] = cvt_pk_bf16(sc[8 * u + 2 * t], sc[8 * u + 2 * t + 1]);
  const float Mh = __builtin_bit_cast(float, __builtin_amdgcn_readlane(__builtin_bit_cast(int, Mc), 4 * (w >> 1)));
  STAMP(5);
  f32x4 oa[8];
#pragma unroll
  for (int dd = 0; dd < 8; ++dd) oa[dd] = f32x4{0.f, 0.f, 0.f, 0.f};
#pragma unroll
  for (int u = 0; u < 2; ++u) {
#pragma unroll
    for (int dd = 0; dd < 8; ++dd) {
      v4u bf;
#pragma unroll
      for (int t = 0; t < 4; ++t)
        bf[t] = (dd & 1) ? perm_hi(V[8 * u + 2 * t][dd >> 1], V[8 * u + 2 * t + 1][dd >> 1]) : perm_lo(V[8 * u + 2 * t][dd >> 1], V[8 * u + 2 * t + 1][dd >> 1]);
      oa[dd] = mfma32(pa[u], bf, oa[dd]);
    }
  }
  *(f32x4*)&red[(w * 4 + g) * 128 + 8 * c] = f32x4{oa[0][0], oa[1][0], oa[2][0], oa[3][0]};
  *(f32x4*)&red[(w * 4 + g) * 128 + 8 * c + 4] = f32x4{oa[4][0], oa[5][0], oa[6][0], oa[7][0]};
  STAMP(6);
  WG_BARRIER();
  {
    const int h = w >> 1;
    float s = red[tid];
#pragma unroll
    for (int ww = 1; ww < 8; ++ww) s += red[ww * 512 + tid];
    const f32x4 l0 = *(const f32x4*)&ls[h * 8], l1 = *(const f32x4*)&ls[h * 8 + 4];
    const float L = ((l0[0] + l0[1]) + (l0[2] + l0[3])) + ((l1[0] + l1[1]) + (l1[2] + l1[3]));
    s *= __builtin_amdgcn_rcpf(L);
    const unsigned p01 = cvt_pk_bf16(dpp<0x00>(s), dpp<0x55>(s)), p23 = cvt_pk_bf16(dpp<0xAA>(s), dpp<0xFF>(s));
    const int rowi = ((grp * 4 + h) * 8 + b) * 2 + half;
    if ((lane & 3) == 0)
      __builtin_amdgcn_raw_buffer_store_b128(v4u{p01, tagA, p23, tagB}, rws, WS_OG + rowi * 512 + (w & 1) * 256 + lane * 4, 0, AUX_SC1);
    if ((w & 1) == 0 && lane == 0) {
      const unsigned lse = __float_as_uint(Mh + __builtin_amdgcn_logf(L));
      __builtin_amdgcn_raw_buffer_store_b128(v4u{lse, tagA, lse, tagB}, rws, WS_LG + rowi * 16, 0, AUX_SC1);
    }
  }
  STAMP(7);
  {
    const int rowp = (grp * 4 + hq) * 8 + w;
    const int ooff = WS_OG + rowp * 1024 + lane * 16;
    const int loff = WS_LG + rowp * 32 + (lane >> 5) * 16;
    v4u o, e;
    unsigned spins = 0;
    for (;;) {
      asm volatile("" ::: "memory");
      o = __builtin_amdgcn_raw_buffer_load_b128(rws, ooff, 0, AUX_SC1);
      e = __builtin_amdgcn_raw_buffer_load_b128(rws, loff, 0, AUX_SC1);
      if (__all(((o[1] ^ tagA) | (o[3] ^ tagB) | (e[1] ^ tagA) | (e[3] ^ tagB)) == 0u)) break;
      if (++spins > SPIN_CAP) break;
      __builtin_amdgcn_s_sleep(1);
    }
    STAMPV(13, spins);
    STAMP(8);
    const auto es = __builtin_amdgcn_permlane32_swap(e[0], e[0], false, false);
    const float e0 = __uint_as_float(es[0]), e1 = __uint_as_float(es[1]), own = __uint_as_float(e[0]);
    const float me = fmaxf(e0, e1);
    const float a = __builtin_amdgcn_exp2f(own - me) * __builtin_amdgcn_rcpf(__builtin_amdgcn_exp2f(e0 - me) + __builtin_amdgcn_exp2f(e1 - me));
    float v[4] = {a * __uint_as_float(o[0] << 16), a * __uint_as_float(o[0] & 0xFFFF0000u), a * __uint_as_float(o[2] << 16), a * __uint_as_float(o[2] & 0xFFFF0000u)};
#pragma unroll
    for (int j = 0; j < 4; ++j) {
      const auto t = __builtin_amdgcn_permlane32_swap(__float_as_uint(v[j]), __float_as_uint(v[j]), false, false);
      v[j] = __uint_as_float(t[0]) + __uint_as_float(t[1]);
    }
    if (lane < 32) *(v2u*)&oa_s[w * 72 + 2 * lane] = v2u{cvt_pk_bf16(v[0], v[1]), cvt_pk_bf16(v[2], v[3])};
  }
  v2u wb[8];
#pragma unroll
  for (int dd = 0; dd < 8; ++dd) {
    wb[dd][0] = (dd & 1) ? perm_hi(wo[0][dd >> 1], wo[1][dd >> 1]) : perm_lo(wo[0][dd >> 1], wo[1][dd >> 1]);
    wb[dd][1] = (dd & 1) ? perm_hi(wo[2][dd >> 1], wo[3][dd >> 1]) : perm_lo(wo[2][dd >> 1], wo[3][dd >> 1]);
  }
  WG_BARRIER();
  {
    const v2u af = *(const v2u*)&oa_s[(c & 7) * 72 + 8 * w + 2 * g];
    f32x4 d[8];
#pragma unroll
    for (int dd = 0; dd < 8; ++dd) d[dd] = mfma16(af, wb[dd], f32x4{0.f, 0.f, 0.f, 0.f});
    if (g < 2) {
#pragma unroll
      for (int r = 0; r < 4; ++r) {
        float* r0 = &red[(w * 8 + 4 * g + r) * 128 + 8 * c];
        *(f32x4*)(r0) = f32x4{d[0][r], d[1][r], d[2][r], d[3][r]};
        *(f32x4*)(r0 + 4) = f32x4{d[4][r], d[5][r], d[6][r], d[7][r]};
      }
    }
  }
  WG_BARRIER();
  {
    f32x2 s = *(const f32x2*)&red[2 * tid];
#pragma unroll
    for (int ww = 1; ww < 8; ++ww) s += *(const f32x2*)&red[ww * 1024 + 2 * tid];
    const int cons = 4 * w + g, prod = kvh * 4 + hq;
    __builtin_amdgcn_raw_buffer_store_b128(v4u{__float_as_uint(s[0]), tagA, __float_as_uint(s[1]), tagB}, rws,
                                           WS_PG + (((br * 4 + cq) * 32 + cons) * 32 + prod) * 256 + c * 16, 0, AUX_SC1);
  }
  STAMP(9);
  if (w == 0) {
    const int jj = kvh * 4 + hq;
    const int poff = WS_PG + ((br * 4 + cq) * 32 + jj) * 8192 + lane * 16;
    v4u pg[8];
    unsigned spins = 0;
    for (;;) {
      asm volatile("" ::: "memory");
      unsigned bad = 0;
#pragma unroll
      for (int i = 0; i < 8; ++i) pg[i] = __builtin_amdgcn_raw_buffer_load_b128(rws, poff + (i & 3) * 1024, (i >> 2) * 4096, AUX_SC1);
#pragma unroll
      for (int i = 0; i < 8; ++i) bad |= (pg[i][1] ^ tagA) | (pg[i][3] ^ tagB);
      if (__all(bad == 0u)) break;
      if (++spins > SPIN_CAP) break;
      __builtin_amdgcn_s_sleep(1);
    }
    STAMPV(14, spins);
    float s0 = 0.f, s1 = 0.f;
#pragma unroll
    for (int i = 0; i < 8; ++i) { s0 += __uint_as_float(pg[i][0]); s1 += __uint_as_float(pg[i][2]); }
    s0 = xrow_sum(s0);
    s1 = xrow_sum(s1);
    if (lane < 16) {
      const int bb = jj >> 2, sub = jj & 3;
      ((unsigned*)p.out)[(bb * 1024 + br * 512 + 128 * cq + 32 * sub) / 2 + c] = cvt_pk_bf16(s0, s1);
    }
    if (jj == 0 && cq == 0 && lane == 0)
      __builtin_amdgcn_raw_buffer_store_b64(v2u{(unsigned)T, (unsigned)(T >> 32)}, rws, WS_EPOCH + br * 256, 0, AUX_SC1);
  }
  STAMP(10);
  STAMP_DUMP();
}

extern "C" void kernel_launch(void* const* d_in, const int* in_sizes, int n_in, void* d_out, int out_size, void* d_ws, size_t ws_size,
                              hipStream_t stream) {
  Params p;
  p.x[0] = d_in[0]; p.k[0] = d_in[1]; p.v[0] = d_in[2]; p.wq[0] = d_in[3]; p.wo[0] = d_in[4];
  p.x[1] = d_in[5]; p.k[1] = d_in[6]; p.v[1] = d_in[7]; p.wq[1] = d_in[8]; p.wo[1] = d_in[9];
  p.out = d_out; p.ws = d_ws;
  hipLaunchKernelGGL(fused_decode, dim3(256), dim3(512), 0, stream, p);
}
```

```cpp
#include <hip/hip_runtime.h>
#include <hip/hip_bf16.h>
#include <stdint.h>

typedef unsigned v4u __attribute__((ext_vector_type(4)));
typedef unsigned v2u __attribute__((ext_vector_type(2)));
typedef unsigned v16u __attribute__((ext_vector_type(16)));
typedef float f32x4 __attribute__((ext_vector_type(4)));
typedef float f32x2 __attribute__((ext_vector_type(2)));
typedef __bf16 bf16x8 __attribute__((ext_vector_type(8)));
typedef unsigned long long u64;

constexpr int AUX_NT = 2, AUX_SC1 = 16;
constexpr unsigned SPIN_CAP = 1u << 16;
constexpr int PACE_K = 390, PACE_V = 380, PACE_W = 380;
constexpr int PACE_C = 5;
constexpr int RB = 105;
constexpr int STGW = 4, STGC = 1;
constexpr int ODELAY = 28, PDELAY = 20;
constexpr int CPF = 1024, CPT = 16384;

constexpr int WS_EPOCH = 0;
constexpr int WS_TG = 4096;
constexpr int WS_QG = 8192;
constexpr int WS_OG = WS_QG + 8 * 8 * 8192;
constexpr int WS_LG = WS_OG + 8 * 4 * 8 * 1024;
constexpr int WS_PG = WS_LG + 8 * 4 * 256;
constexpr int WS_END = WS_PG + 4 * 32 * 32 * 128;
constexpr int WS_BRANCH = 2 << 20;

enum { B_L0 = 11, B_L1 = 13, B_W0 = 9, B_W1 = 10, B_W2 = 17, B_I0 = 15, B_I1 = 8, B_I2 = 16, B_I3 = 12, B_H = 14 };
constexpr int toff(int i) { return ((i & 1) << B_I0) | (((i >> 1) & 1) << B_I1) | (((i >> 2) & 1) << B_I2) | (((i >> 3) & 1) << B_I3); }

struct Desc { const void* base; unsigned bytes; unsigned fmt; };
enum { D_X, D_WQ, D_K, D_WS, D_V, D_WO, D_OUT, D_AUX };
struct Params { Desc d[2][8]; };

__device__ v4u bload128(v4u rsrc, int voff, int soff, int aux) __asm("llvm.amdgcn.raw.buffer.load.v4i32");
__device__ v2u bload64(v4u rsrc, int voff, int soff, int aux) __asm("llvm.amdgcn.raw.buffer.load.v2i32");
__device__ unsigned bload32(v4u rsrc, int voff, int soff, int aux) __asm("llvm.amdgcn.raw.buffer.load.i32");
__device__ void bstore128(v4u data, v4u rsrc, int voff, int soff, int aux) __asm("llvm.amdgcn.raw.buffer.store.v4i32");
__device__ void bstore64(v2u data, v4u rsrc, int voff, int soff, int aux) __asm("llvm.amdgcn.raw.buffer.store.v2i32");
#define DESC(a, i) __builtin_shufflevector(a, a, 4 * (i), 4 * (i) + 1, 4 * (i) + 2, 4 * (i) + 3)
__device__ __forceinline__ f32x4 mfma32(v4u a, v4u b, f32x4 c) {
  return __builtin_amdgcn_mfma_f32_16x16x32_bf16(__builtin_bit_cast(bf16x8, a), __builtin_bit_cast(bf16x8, b), c, 0, 0, 0);
}
__device__ __forceinline__ unsigned perm_lo(unsigned a, unsigned b) { return __builtin_amdgcn_perm(b, a, 0x05040100u); }
__device__ __forceinline__ unsigned perm_hi(unsigned a, unsigned b) { return __builtin_amdgcn_perm(b, a, 0x07060302u); }
__device__ __forceinline__ unsigned cvt_pk_bf16(float lo, float hi) {
  typedef __bf16 bf16x2 __attribute__((ext_vector_type(2)));
  bf16x2 r = {(__bf16)lo, (__bf16)hi};
  return __builtin_bit_cast(unsigned, r);
}
template <int CTRL> __device__ __forceinline__ float dpp(float x) {
  return __builtin_bit_cast(float, __builtin_amdgcn_mov_dpp(__builtin_bit_cast(int, x), CTRL, 0xf, 0xf, true));
}
__device__ __forceinline__ float xrow_max(float x) {
  auto s = __builtin_amdgcn_permlane16_swap(__float_as_uint(x), __float_as_uint(x), false, false);
  x = fmaxf(__uint_as_float(s[0]), __uint_as_float(s[1]));
  auto t = __builtin_amdgcn_permlane32_swap(__float_as_uint(x), __float_as_uint(x), false, false);
  return fmaxf(__uint_as_float(t[0]), __uint_as_float(t[1]));
}
__device__ __forceinline__ float xrow_sum(float x) {
  auto s = __builtin_amdgcn_permlane16_swap(__float_as_uint(x), __float_as_uint(x), false, false);
  x = __uint_as_float(s[0]) + __uint_as_float(s[1]);
  auto t = __builtin_amdgcn_permlane32_swap(__float_as_uint(x), __float_as_uint(x), false, false);
  return __uint_as_float(t[0]) + __uint_as_float(t[1]);
}
__device__ __forceinline__ v4u col_frag(const v4u* r, int dd) {
  v4u f;
#pragma unroll
  for (int t = 0; t < 4; ++t) f[t] = (dd & 1) ? perm_hi(r[2 * t][dd >> 1], r[2 * t + 1][dd >> 1]) : perm_lo(r[2 * t][dd >> 1], r[2 * t + 1][dd >> 1]);
  return f;
}
#define BARRIER_ONLY() do { __builtin_amdgcn_sched_barrier(0); asm volatile("s_barrier" ::: "memory"); __builtin_amdgcn_sched_barrier(0); } while (0)
#define WG_BARRIER() asm volatile("s_waitcnt lgkmcnt(0)\n\ts_barrier" ::: "memory")
#define SCHED_FENCE() __builtin_amdgcn_sched_barrier(0)

__device__ __forceinline__ void scores_softmax(const v4u* K, const unsigned* q_s, float* mx, float* ls, int lane, int w, v4u* pa) {
  const int hl = (lane >> 2) & 3;
  const v4u qf = *(const v4u*)&q_s[hl * 64 + 16 * (lane >> 4) + 4 * (lane & 3)];
  constexpr float C2 = 0.088388347648318440f * 1.4426950408889634f;
  float sc[16];
  f32x4 d[2][4];
#pragma unroll
  for (int j = 0; j < 4; ++j) d[0][j] = mfma32(K[j], qf, f32x4{0.f, 0.f, 0.f, 0.f});
#pragma unroll
  for (int u = 0; u < 4; ++u) {
    SCHED_FENCE();
    if (u < 3) {
#pragma unroll
      for (int j = 0; j < 4; ++j) d[(u + 1) & 1][j] = mfma32(K[4 * u + 4 + j], qf, f32x4{0.f, 0.f, 0.f, 0.f});
    }
#pragma unroll
    for (int j = 0; j < 4; ++j) {
      const f32x4 e = d[u & 1][j];
      sc[4 * u + j] = dpp<0xFF>(e[3]) + (dpp<0xAA>(e[2]) + (dpp<0x55>(e[1]) + dpp<0x00>(e[0])));
    }
  }
  SCHED_FENCE();
  float m = fmaxf(fmaxf(sc[0], sc[1]), sc[2]);
#pragma unroll
  for (int i = 3; i < 15; i += 2) m = fmaxf(fmaxf(m, sc[i]), sc[i + 1]);
  m = xrow_max(fmaxf(m, sc[15])) * C2;
  float l = 0.f;
#pragma unroll
  for (int i = 0; i < 16; ++i) { sc[i] = __builtin_amdgcn_exp2f(fmaf(sc[i], C2, -m)); l += sc[i]; }
  l = xrow_sum(l);
  if (lane < 16 && (lane & 3) == 0) { mx[hl * 8 + w] = m; ls[hl * 8 + w] = l; }
#pragma unroll
  for (int u = 0; u < 2; ++u)
#pragma unroll
    for (int t = 0; t < 4; ++t) pa[u][t] = cvt_pk_bf16(sc[8 * u + 2 * t], sc[8 * u + 2 * t + 1]);
}
__device__ __forceinline__ float wave_scale(const float* mx, const float* ls, int g, int w, float& lse) {
  const f32x4 m0 = *(const f32x4*)&mx[g * 8], m1 = *(const f32x4*)&mx[g * 8 + 4];
  const f32x4 l0 = *(const f32x4*)&ls[g * 8], l1 = *(const f32x4*)&ls[g * 8 + 4];
  const float mo = mx[g * 8 + w];
  const float M = fmaxf(fmaxf(fmaxf(m0[0], m0[1]), fmaxf(m0[2], m0[3])), fmaxf(fmaxf(m1[0], m1[1]), fmaxf(m1[2], m1[3])));
  float L = 0.f;
#pragma unroll
  for (int j = 0; j < 4; ++j) L += l0[j] * __builtin_amdgcn_exp2f(m0[j] - M) + l1[j] * __builtin_amdgcn_exp2f(m1[j] - M);
  lse = M + __builtin_amdgcn_logf(L);
  return __builtin_amdgcn_exp2f(mo - M) * __builtin_amdgcn_rcpf(L);
}

constexpr int sched(int n) {
  return (n <= 160 ? n * PACE_K : n <= 288 ? 160 * PACE_K + (n - 160) * PACE_V : 160 * PACE_K + 128 * PACE_V + (n - 288) * PACE_W) / 100 + PACE_C;
}

__global__ void __launch_bounds__(512, 2) fused_decode(const void* a_ws, const void* a_x0, const void* a_wq0, const void* a_k0, const void* a_x1, const void* a_wq1, const void* a_k1, u64 a_z, Params p) {
  __shared__ __attribute__((aligned(16))) float lds[4 * 8 * 128 + 8 * 512 + 256 + 64 + 12 + 4 * 68 * 4];
  float* red = lds;
  float* redo = lds + 4 * 8 * 128;
  unsigned* q_s = (unsigned*)(redo + 8 * 512);
  float* mx = redo + 8 * 512 + 256;
  float* ls = mx + 32;
  unsigned* cnt = (unsigned*)(ls + 32);
  v4u* a_s = (v4u*)(cnt + 12);

  const int bid = blockIdx.x;
  v16u ka, kb;
  u64 tt0;
  asm volatile("s_load_dwordx16 %0, %3, %4 offset:64\n\ts_load_dwordx16 %1, %3, %4 offset:128\n\ts_memrealtime %2"
               : "=&s"(ka), "=&s"(kb), "=&s"(tt0) : "s"(__builtin_amdgcn_kernarg_segment_ptr()), "s"(bid & 128));
  SCHED_FENCE();
  const int tid = threadIdx.x, lane = tid & 63, w = __builtin_amdgcn_readfirstlane(tid >> 6);
  unsigned cpf;
  {
    u64 pc;
    unsigned len;
    asm volatile("s_getpc_b64 %0\n.Lfd_pc:\n\ts_mov_b32 %1, .Lfd_end-.Lfd_pc" : "=s"(pc), "=s"(len));
    const unsigned x = tid ^ 256, co = ((bid >> 3) & (CPT / CPF - 1)) * CPF;
    cpf = bload32(v4u{(unsigned)pc, (unsigned)(pc >> 32), len, 0x00020000u}, co + ((x << 6) | ((x / (CPF / 64)) << 16)), 0, 0);
  }
  SCHED_FENCE();
  const int kvh = (bid >> 3) & 7, mem = (bid & 7) * 2 + ((bid >> 6) & 1);
  const int g = lane >> 4, c = lane & 15;
  const int b = mem >> 1, half = mem & 1, hq = mem >> 2, cq = (mem ^ kvh) & 3;
  const int jj = kvh * 4 + hq;
  const int w4 = w & 3;
  const int kvbase = ((b * 8 + kvh) * 4096 + 3072) * 256 + (half << B_H) + ((w & 1) << B_W0) + (((w >> 1) & 1) << B_W1) + ((w >> 2) << B_W2);
  int koff = kvbase + (((lane >> 2) & 1) << B_L0) + (((lane >> 3) & 1) << B_L1) + 64 * g + 16 * (lane & 3);
  const int voff = kvbase + ((g & 1) << B_L0) + ((g >> 1) << B_L1) + 16 * c;
  const int memq = mem ^ (kvh << 1);
  int qwoff = ((256 * half + 64 * w + 8 * (lane >> 3)) * 4096 + kvh * 512 + 64 * (memq >> 1) + 8 * (lane & 7)) * 2;
  int xoff = ((c & 7) * 512 + 256 * half + 64 * w + 16 * g + 8 * (c >> 3)) * 2;
  if (tid < 10) cnt[tid] = tid == 8 ? ~0u : 0u;
  SCHED_FENCE();
  asm volatile("" : "+v"(koff), "+v"(qwoff), "+v"(xoff));
  asm volatile("s_waitcnt lgkmcnt(0)" : "+s"(ka), "+s"(kb), "+s"(tt0) :: "memory");
  SCHED_FENCE();
  const v4u rk = DESC(ka, D_K), rws = DESC(ka, D_WS);
  v4u K[16], V[16], pa[2], wq[8], xa;
#define rv DESC(kb, D_V - 4)
#define rwo DESC(kb, D_WO - 4)
  const unsigned lbase = (unsigned)(uintptr_t)(__attribute__((address_space(3))) float*)lds;
  f32x4 oa[8];
  unsigned t0 = (unsigned)tt0;

#define LOAD_K(i) K[i] = bload128(rk, koff, toff(i), AUX_NT)
#define LOAD_V(i) V[i] = bload128(rv, voff, toff(i), AUX_NT)
#define PACED(n) do { SCHED_FENCE(); while ((unsigned)__builtin_amdgcn_s_memrealtime() - t0 - stg < (unsigned)sched(n)) {} SCHED_FENCE(); } while (0)
#define TAGS(ep) const u64 T = (((u64)__builtin_amdgcn_readfirstlane(ep[1]) << 32) | (u64)__builtin_amdgcn_readfirstlane(ep[0])) + 1ull; \
                 const unsigned tagA = (unsigned)T, tagH = (unsigned)(T >> 32), tagB = tagH ^ tagA
#define CNT_OFF(i) ((4 * 8 * 128 + 8 * 512 + 256 + 64 + (i)) * 4)
#define ARRIVE_AT(a, i) asm volatile("s_mov_b64 exec, 1\n\tds_add_u32 %0, %1 offset:%2\n\ts_mov_b64 exec, -1" :: "v"(a), "v"(1u), "n"(CNT_OFF(i)) : "memory")
#define ARRIVE(i) ARRIVE_AT(lbase, i)
#define AWAIT(i, n) do { asm volatile("" ::: "memory"); \
    while (__builtin_amdgcn_readfirstlane(__hip_atomic_load(&cnt[i], __ATOMIC_RELAXED, __HIP_MEMORY_SCOPE_WORKGROUP)) < (n)) {} \
    asm volatile("" ::: "memory"); } while (0)
#define COMMON_START() do { const unsigned n_ = (unsigned)__builtin_amdgcn_readfirstlane(__hip_atomic_load(&cnt[C_S], __ATOMIC_RELAXED, __HIP_MEMORY_SCOPE_WORKGROUP)); \
    asm volatile("" ::: "memory"); \
    const u64 ms_ = __hip_atomic_load((u64*)&cnt[8], __ATOMIC_RELAXED, __HIP_MEMORY_SCOPE_WORKGROUP); \
    const unsigned m_ = (unsigned)__builtin_amdgcn_readfirstlane((unsigned)ms_), s_ = (unsigned)__builtin_amdgcn_readfirstlane((unsigned)(ms_ >> 32)); \
    const int a_ = (int)(s_ - (m_ << 4)) >> 4;   \
    const int d_ = (int)(t0 - m_) - a_; \
    t0 -= n_ < 4 ? 0u : (unsigned)min(max(d_, -150), 150); } while (0)
#define MERGE_O() do { \
    *(f32x4*)&redo[(w * 4 + g) * 128 + 8 * c] = f32x4{oa[0][0], oa[1][0], oa[2][0], oa[3][0]} * fsc; \
    *(f32x4*)&redo[(w * 4 + g) * 128 + 8 * c + 4] = f32x4{oa[4][0], oa[5][0], oa[6][0], oa[7][0]} * fsc; \
    ARRIVE(C_M); \
  } while (0)
#define R2(i) (*(const f32x2*)(r0 + 512 * (i)))
  enum { C_Q, C_S, C_Y, C_M, C_A };

  if (w < 4) {
    xa = bload128(DESC(ka, D_X), xoff, 0, 0);
    SCHED_FENCE();
#pragma unroll
    for (int i = 0; i < 8; ++i) wq[i] = bload128(DESC(ka, D_WQ), qwoff, i * 8192, AUX_NT);
    SCHED_FENCE();
  } else {
#pragma unroll
    for (int i = 0; i < 8; ++i) asm volatile("" : "=v"(wq[i]));
  }
  const v2u ep = bload64(rws, WS_EPOCH, 0, AUX_SC1);
  BARRIER_ONLY();
  SCHED_FENCE();
  {
    const unsigned t1 = (unsigned)__builtin_amdgcn_s_memrealtime();
    t0 += (unsigned)max((int)(t1 - t0) - RB, 0);
    SCHED_FENCE();
  }
  SCHED_FENCE();
  const unsigned stg = (unsigned)(w4 * STGW + (mem & 3) * STGC - (3 * STGW + 3 * STGC) / 2);
  int wr = w;
  asm volatile("" : "+s"(wr));
  if (__builtin_expect(wr < 4, 1)) {
    {
      f32x4 d[8];
#pragma unroll
      for (int dd = 0; dd < 8; ++dd) d[dd] = mfma32(xa, col_frag(wq, dd), f32x4{0.f, 0.f, 0.f, 0.f});
      if ((g >> 1) == (c >> 3)) {
#pragma unroll
        for (int r = 0; r < 4; ++r) {
          float* r0 = &red[((2 * w + (g >> 1)) * 8 + 4 * (g & 1) + r) * 64 + 8 * (c & 7)];
          *(f32x4*)(r0) = f32x4{d[0][r], d[1][r], d[2][r], d[3][r]};
          *(f32x4*)(r0 + 4) = f32x4{d[4][r], d[5][r], d[6][r], d[7][r]};
        }
      }
    }
    ARRIVE(C_Q);
    TAGS(ep);
    (void)tagH;
    AWAIT(C_Q, 4);
    {
      const float* r0 = &red[2 * tid];
      const f32x2 s = ((R2(0) + R2(1)) + (R2(2) + R2(3))) + ((R2(4) + R2(5)) + (R2(6) + R2(7)));
      bstore128(v4u{__float_as_uint(s[0]), tagA, __float_as_uint(s[1]), tagB}, rws,
                WS_QG + ((kvh * 8 + (tid >> 5)) * 16 + memq) * 512 + (lane & 31) * 16, 0, AUX_SC1);
      if (tid == 0) bstore128(v4u{t0, tagA, t0, tagB}, rws, WS_TG + (kvh * 16 + mem) * 16, 0, AUX_SC1);
    }
    SCHED_FENCE();
    asm volatile("" :: "v"(cpf));
    SCHED_FENCE();
#pragma unroll
    for (int i = 0; i < 16; ++i) { PACED(96 + 4 * i); LOAD_K(i); }
    SCHED_FENCE();
    COMMON_START();
#pragma unroll
    for (int i = 0; i < 16; ++i) { PACED(160 + 4 * i); LOAD_V(i); }
    SCHED_FENCE();
    AWAIT(C_S, 4);
    scores_softmax(K, q_s, mx, ls, lane, w, pa);
    ARRIVE(C_Y);
    SCHED_FENCE();
    AWAIT(C_Y, 8);
    {
      float lse;
      const float fsc = wave_scale(mx, ls, g, w, lse);
      if (lane == 16 * w)
        bstore128(v4u{__float_as_uint(lse), tagA, __float_as_uint(lse), tagB}, rws, WS_LG + ((kvh * 4 + w) * 16 + mem) * 16, 0, AUX_SC1);
#pragma unroll
      for (int dd = 0; dd < 8; ++dd) oa[dd] = mfma32(pa[0], col_frag(V, dd), f32x4{0.f, 0.f, 0.f, 0.f});
#pragma unroll
      for (int dd = 0; dd < 8; ++dd) oa[dd] = mfma32(pa[1], col_frag(V + 8, dd), oa[dd]);
      MERGE_O();
    }
    SCHED_FENCE();
    COMMON_START();
    v4u wo[8];
    {
      const int off = ((jj * 128 + 32 * w + 4 * g) * 512 + 128 * cq + 8 * c) * 2;
#pragma unroll
      for (int i = 0; i < 8; ++i) { PACED(288 + 4 * i); wo[i] = bload128(rwo, off + (i & 3) * 1024, (i >> 2) * 16384, AUX_NT); }
    }
    SCHED_FENCE();
    {
      const int loff = WS_LG + ((kvh * 4 + hq) * 16 + 4 * g + (c & 3)) * 16;
      v4u e = bload128(rws, loff, 0, AUX_SC1);
      SCHED_FENCE();
      v4u wb[8];
#pragma unroll
      for (int dd = 0; dd < 8; ++dd) { wb[dd] = col_frag(wo, dd); asm volatile("" : "+v"(wb[dd])); }
      SCHED_FENCE();
      unsigned spins = 0;
#pragma nounroll
      for (;;) {
        const bool be = ((e[1] ^ tagA) | (e[3] ^ tagB)) != 0u;
        if (!__any(be) || ++spins >= SPIN_CAP) break;
        asm volatile("" ::: "memory");
        if (be) e = bload128(rws, loff, 0, AUX_SC1);
      }
      const float eo = __uint_as_float(e[0]);
      const float wt = __builtin_amdgcn_rcpf(1.f + __builtin_amdgcn_exp2f(dpp<0xB1>(eo) - eo));
      float w0 = dpp<0x00>(wt), w1 = dpp<0x55>(wt), w2 = dpp<0xAA>(wt), w3 = dpp<0xFF>(wt);
      asm volatile("" : "+v"(w0), "+v"(w1), "+v"(w2), "+v"(w3));
      SCHED_FENCE();
      AWAIT(C_A + w, 1);
      const v4u af = a_s[w * 68 + g * 17 + c];
      f32x4 d[8];
#pragma unroll
      for (int dd = 0; dd < 8; ++dd) d[dd] = mfma32(af, wb[dd], f32x4{0.f, 0.f, 0.f, 0.f});
      float* r0 = &red[(w * 8 + 2 * g) * 128 + 8 * c];
#pragma unroll
      for (int j = 0; j < 2; ++j) {
        f32x4 y0, y1;
#pragma unroll
        for (int dd = 0; dd < 4; ++dd) {
          y0[dd] = fmaf(w0, d[4 * j + dd][0], w1 * d[4 * j + dd][1]);
          y1[dd] = fmaf(w2, d[4 * j + dd][2], w3 * d[4 * j + dd][3]);
        }
        *(f32x4*)(r0 + 4 * j) = y0;
        *(f32x4*)(r0 + 128 + 4 * j) = y1;
      }
    }
    WG_BARRIER();
    {
      const float* r0 = &red[2 * w * 128 + 4 * lane];
      const f32x4 s = (*(const f32x4*)r0 + *(const f32x4*)(r0 + 1024)) + (*(const f32x4*)(r0 + 2048) + *(const f32x4*)(r0 + 3072));
      bstore128(v4u{cvt_pk_bf16(s[0], s[1]), tagA, cvt_pk_bf16(s[2], s[3]), tagB}, rws,
                WS_PG + ((cq * 32 + 8 * w + (lane >> 3)) * 32 + jj) * 128 + (lane & 7) * 16, 0, AUX_SC1);
    }
    return;
  }

  LOAD_K(0);
#pragma unroll
  for (int i = 1; i < 16; ++i) { PACED(32 + 4 * i); LOAD_K(i); }
  SCHED_FENCE();
  TAGS(ep);
  {
    const int qoff = WS_QG + ((kvh * 8 + b) * 4 + w4) * 2048 + lane * 16;
    const int tgoff = WS_TG + (kvh * 16 + 4 * w4 + (lane & 3)) * 16;
    v4u q0 = bload128(rws, qoff, 0, AUX_SC1);
    v4u q1 = bload128(rws, qoff + 1024, 0, AUX_SC1);
    v4u tq = {t0, tagA, t0, tagB};
    if (lane < 4) tq = bload128(rws, tgoff, 0, AUX_SC1);
    unsigned spins = 0;
#pragma nounroll
    for (;;) {
      const bool ba = ((q0[1] ^ tagA) | (q0[3] ^ tagB)) != 0u;
      const bool bb = ((q1[1] ^ tagA) | (q1[3] ^ tagB)) != 0u;
      const bool bt = ((tq[1] ^ tagA) | (tq[3] ^ tagB)) != 0u;
      if (!__any(ba || bb || bt) || ++spins >= SPIN_CAP) break;
      asm volatile("" ::: "memory");
      if (ba) q0 = bload128(rws, qoff, 0, AUX_SC1);
      if (bb) q1 = bload128(rws, qoff + 1024, 0, AUX_SC1);
      if (bt) tq = bload128(rws, tgoff, 0, AUX_SC1);
    }
    asm volatile("" :: "v"(cpf));
    const auto u0 = __builtin_amdgcn_permlane32_swap(q0[0], q1[0], false, false);
    const auto u1 = __builtin_amdgcn_permlane32_swap(q0[2], q1[2], false, false);
    q_s[w4 * 64 + lane] = cvt_pk_bf16(__uint_as_float(u0[0]) + __uint_as_float(u0[1]), __uint_as_float(u1[0]) + __uint_as_float(u1[1]));
    asm volatile("s_mov_b64 exec, 15\n\tds_min_u32 %0, %1 offset:%2\n\ts_mov_b64 exec, -1" :: "v"(lbase), "v"(tq[0]), "n"(CNT_OFF(8)) : "memory");
    asm volatile("s_mov_b64 exec, 15\n\tds_add_u32 %0, %1 offset:%2\n\ts_mov_b64 exec, -1" :: "v"(lbase), "v"(tq[0]), "n"(CNT_OFF(9)) : "memory");
  }
  ARRIVE(C_S);
  AWAIT(C_S, 4);
  COMMON_START();
  scores_softmax(K, q_s, mx, ls, lane, w, pa);
  ARRIVE(C_Y);
  SCHED_FENCE();
#pragma unroll
  for (int i = 0; i < 16; ++i) { PACED(224 + 4 * i); LOAD_V(i); }
  SCHED_FENCE();
  AWAIT(C_Y, 8);
  {
    float lse;
    float fsc = wave_scale(mx, ls, g, w, lse);
    asm volatile("" : "+v"(fsc));
    SCHED_FENCE();
#pragma unroll
    for (int dd = 0; dd < 8; ++dd) oa[dd] = mfma32(pa[0], col_frag(V, dd), f32x4{0.f, 0.f, 0.f, 0.f});
    SCHED_FENCE();
#pragma unroll
    for (int dd = 0; dd < 8; ++dd) oa[dd] = mfma32(pa[1], col_frag(V + 8, dd), oa[dd]);
    MERGE_O();
  }
  AWAIT(C_M, 8);
  {
    const float* r0 = &redo[2 * (tid - 256)];
    const f32x2 s = ((R2(0) + R2(1)) + (R2(2) + R2(3))) + ((R2(4) + R2(5)) + (R2(6) + R2(7)));
    const unsigned p01 = cvt_pk_bf16(s[0], s[1]);
    const unsigned p23 = (unsigned)__builtin_amdgcn_mov_dpp((int)p01, 0xF5, 0xf, 0xf, true);
    if (!(lane & 1))
      bstore128(v4u{p01, tagA, p23, tagB}, rws, WS_OG + ((kvh * 4 + w4) * 16 + mem) * 512 + (lane >> 1) * 16, 0, AUX_SC1);
  }
  {
    const int ooff = WS_OG + ((kvh * 4 + hq) * 16 + (lane >> 2)) * 512 + w4 * 128 + (lane & 3) * 16;
    __builtin_amdgcn_s_sleep(ODELAY);
    v4u o0 = bload128(rws, ooff, 0, AUX_SC1);
    v4u o1 = bload128(rws, ooff + 64, 0, AUX_SC1);
    unsigned spins = 0;
#pragma nounroll
    for (;;) {
      const bool bo = (((o0[1] ^ tagA) | (o0[3] ^ tagB)) | ((o1[1] ^ tagA) | (o1[3] ^ tagB))) != 0u;
      if (!__any(bo) || ++spins >= SPIN_CAP) break;
      asm volatile("" ::: "memory");
      if (bo) {
        o0 = bload128(rws, ooff, 0, AUX_SC1);
        o1 = bload128(rws, ooff + 64, 0, AUX_SC1);
      }
    }
    a_s[w4 * 68 + (lane & 3) * 17 + (lane >> 2)] = v4u{o0[0], o0[2], o1[0], o1[2]};
    ARRIVE_AT(lbase + 4 * w4, C_A);
  }
  if (w4 >= 2) { BARRIER_ONLY(); return; }
  const int poff = WS_PG + (cq * 32 + jj) * 4096 + (lane >> 2) * 128 + w4 * 64 + (lane & 3) * 16;
  BARRIER_ONLY();
  __builtin_amdgcn_s_sleep(PDELAY);
  v4u pg0 = bload128(rws, poff, 0, AUX_SC1);
  v4u pg1 = bload128(rws, poff + 2048, 0, AUX_SC1);
  bool b0 = ((pg0[1] ^ tagA) | (pg0[3] ^ tagB)) != 0u, b1 = ((pg1[1] ^ tagA) | (pg1[3] ^ tagB)) != 0u;
  unsigned spins = 0;
#pragma nounroll
  while (__any(b0 || b1) && ++spins < SPIN_CAP) {
    asm volatile("" ::: "memory");
    if (b0) pg0 = bload128(rws, poff, 0, AUX_SC1);
    if (b1) pg1 = bload128(rws, poff + 2048, 0, AUX_SC1);
    b0 = ((pg0[1] ^ tagA) | (pg0[3] ^ tagB)) != 0u;
    b1 = ((pg1[1] ^ tagA) | (pg1[3] ^ tagB)) != 0u;
  }
  float sv[4] = {__uint_as_float(pg0[0] << 16) + __uint_as_float(pg1[0] << 16), __uint_as_float(pg0[0] & 0xffff0000u) + __uint_as_float(pg1[0] & 0xffff0000u),
                 __uint_as_float(pg0[2] << 16) + __uint_as_float(pg1[2] << 16), __uint_as_float(pg0[2] & 0xffff0000u) + __uint_as_float(pg1[2] & 0xffff0000u)};
#pragma unroll
  for (int i = 0; i < 4; ++i) { sv[i] += dpp<0x124>(sv[i]); sv[i] += dpp<0x128>(sv[i]); sv[i] = xrow_sum(sv[i]); }
  if (lane < 4) bstore64(v2u{cvt_pk_bf16(sv[0], sv[1]), cvt_pk_bf16(sv[2], sv[3])}, DESC(kb, D_OUT - 4), ((jj >> 2) * 1024 + 128 * cq + 32 * (jj & 3) + 16 * w4) * 2 + lane * 8, 0, AUX_SC1);
  if (jj == 0 && cq == 0 && tid == 256)
    bstore64(v2u{tagA, tagH}, rws, WS_EPOCH, 0, AUX_SC1);
  asm volatile(".Lfd_end:");
}

extern "C" void kernel_launch(void* const* d_in, const int* in_sizes, int n_in, void* d_out, int out_size, void* d_ws, size_t ws_size,
                              hipStream_t stream) {
  constexpr unsigned FMT = 0x00020000u;
  Params p;
  for (int br = 0; br < 2; ++br) {
    void* const* in = d_in + 5 * br;
    p.d[br][D_X] = Desc{in[0], 8 * 512 * 2, FMT};
    p.d[br][D_K] = Desc{in[1], 8 * 8 * 4096 * 128 * 2, FMT};
    p.d[br][D_V] = Desc{in[2], 8 * 8 * 4096 * 128 * 2, FMT};
    p.d[br][D_WQ] = Desc{in[3], 512 * 4096 * 2, FMT};
    p.d[br][D_WO] = Desc{in[4], 512 * 4096 * 2, FMT};
    p.d[br][D_WS] = Desc{(char*)d_ws + br * WS_BRANCH, WS_END, FMT};
    p.d[br][D_OUT] = Desc{(char*)d_out + br * 1024, 16384u - br * 1024, FMT};
    p.d[br][D_AUX] = Desc{(char*)d_ws + (8 << 20), 1 << 20, FMT};
  }
  hipLaunchKernelGGL(fused_decode, dim3(256), dim3(512), 0, stream, (const void*)d_ws, (const void*)d_in[0], (const void*)d_in[3], (const void*)d_in[1],
                     (const void*)d_in[5], (const void*)d_in[8], (const void*)d_in[6], 0ull, p);
}
```

```cpp
#include <hip/hip_runtime.h>
#include <hip/hip_bf16.h>
#include <stdint.h>

typedef unsigned v4u __attribute__((ext_vector_type(4)));
typedef unsigned v2u __attribute__((ext_vector_type(2)));
typedef unsigned v16u __attribute__((ext_vector_type(16)));
typedef float f32x4 __attribute__((ext_vector_type(4)));
typedef float f32x2 __attribute__((ext_vector_type(2)));
typedef __bf16 bf16x8 __attribute__((ext_vector_type(8)));
typedef unsigned long long u64;

constexpr int AUX_NT = 2, AUX_SC1 = 16;
constexpr unsigned SPIN_CAP = 1u << 20;
constexpr int PACE_K = 390, PACE_V = 380, PACE_W = 380;
constexpr int PACE_C = 5;
constexpr int RB = 105;
constexpr int STGW = 4, STGC = 1;
constexpr int ODELAY = 28, PDELAY = 20;
constexpr int CPF = 1024, CPT = 16384;

constexpr int WS_EPOCH = 0;
constexpr int WS_TG = 4096;
constexpr int WS_QG = 8192;
constexpr int WS_OG = WS_QG + 8 * 8 * 8192;
constexpr int WS_LG = WS_OG + 8 * 4 * 8 * 1024;
constexpr int WS_PG = WS_LG + 8 * 4 * 256;
constexpr int WS_END = WS_PG + 4 * 32 * 32 * 128;
constexpr int WS_BRANCH = 2 << 20;

enum { B_L0 = 11, B_L1 = 13, B_W0 = 9, B_W1 = 10, B_W2 = 17, B_I0 = 15, B_I1 = 8, B_I2 = 16, B_I3 = 12, B_H = 14 };
constexpr int toff(int i) { return ((i & 1) << B_I0) | (((i >> 1) & 1) << B_I1) | (((i >> 2) & 1) << B_I2) | (((i >> 3) & 1) << B_I3); }

struct Desc { const void* base; unsigned bytes; unsigned fmt; };
enum { D_X, D_WQ, D_K, D_WS, D_V, D_WO, D_OUT, D_NONE };
struct Params { Desc d[2][8]; };

__device__ v4u bload128(v4u rsrc, int voff, int soff, int aux) __asm("llvm.amdgcn.raw.buffer.load.v4i32");
__device__ v2u bload64(v4u rsrc, int voff, int soff, int aux) __asm("llvm.amdgcn.raw.buffer.load.v2i32");
__device__ unsigned bload32(v4u rsrc, int voff, int soff, int aux) __asm("llvm.amdgcn.raw.buffer.load.i32");
__device__ void bstore128(v4u data, v4u rsrc, int voff, int soff, int aux) __asm("llvm.amdgcn.raw.buffer.store.v4i32");
__device__ void bstore64(v2u data, v4u rsrc, int voff, int soff, int aux) __asm("llvm.amdgcn.raw.buffer.store.v2i32");
#define DESC(a, i) __builtin_shufflevector(a, a, 4 * (i), 4 * (i) + 1, 4 * (i) + 2, 4 * (i) + 3)
__device__ __forceinline__ f32x4 mfma32(v4u a, v4u b, f32x4 c) {
  return __builtin_amdgcn_mfma_f32_16x16x32_bf16(__builtin_bit_cast(bf16x8, a), __builtin_bit_cast(bf16x8, b), c, 0, 0, 0);
}
__device__ __forceinline__ unsigned perm_lo(unsigned a, unsigned b) { return __builtin_amdgcn_perm(b, a, 0x05040100u); }
__device__ __forceinline__ unsigned perm_hi(unsigned a, unsigned b) { return __builtin_amdgcn_perm(b, a, 0x07060302u); }
__device__ __forceinline__ unsigned cvt_pk_bf16(float lo, float hi) {
  typedef __bf16 bf16x2 __attribute__((ext_vector_type(2)));
  bf16x2 r = {(__bf16)lo, (__bf16)hi};
  return __builtin_bit_cast(unsigned, r);
}
template <int CTRL> __device__ __forceinline__ float dpp(float x) {
  return __builtin_bit_cast(float, __builtin_amdgcn_mov_dpp(__builtin_bit_cast(int, x), CTRL, 0xf, 0xf, true));
}
__device__ __forceinline__ float xrow_max(float x) {
  auto s = __builtin_amdgcn_permlane16_swap(__float_as_uint(x), __float_as_uint(x), false, false);
  x = fmaxf(__uint_as_float(s[0]), __uint_as_float(s[1]));
  auto t = __builtin_amdgcn_permlane32_swap(__float_as_uint(x), __float_as_uint(x), false, false);
  return fmaxf(__uint_as_float(t[0]), __uint_as_float(t[1]));
}
__device__ __forceinline__ float xrow_sum(float x) {
  auto s = __builtin_amdgcn_permlane16_swap(__float_as_uint(x), __float_as_uint(x), false, false);
  x = __uint_as_float(s[0]) + __uint_as_float(s[1]);
  auto t = __builtin_amdgcn_permlane32_swap(__float_as_uint(x), __float_as_uint(x), false, false);
  return __uint_as_float(t[0]) + __uint_as_float(t[1]);
}
__device__ __forceinline__ v4u col_frag(const v4u* r, int dd) {
  v4u f;
#pragma unroll
  for (int t = 0; t < 4; ++t) f[t] = (dd & 1) ? perm_hi(r[2 * t][dd >> 1], r[2 * t + 1][dd >> 1]) : perm_lo(r[2 * t][dd >> 1], r[2 * t + 1][dd >> 1]);
  return f;
}
#define BARRIER_ONLY() do { __builtin_amdgcn_sched_barrier(0); asm volatile("s_barrier" ::: "memory"); __builtin_amdgcn_sched_barrier(0); } while (0)
#define WG_BARRIER() asm volatile("s_waitcnt lgkmcnt(0)\n\ts_barrier" ::: "memory")
#define SCHED_FENCE() __builtin_amdgcn_sched_barrier(0)

__device__ __forceinline__ void scores_softmax(const v4u* K, const unsigned* q_s, float* mx, float* ls, int lane, int w, v4u* pa) {
  const int hl = (lane >> 2) & 3;
  const v4u qf = *(const v4u*)&q_s[hl * 64 + 16 * (lane >> 4) + 4 * (lane & 3)];
  constexpr float C2 = 0.088388347648318440f * 1.4426950408889634f;
  float sc[16];
  f32x4 d[2][4];
#pragma unroll
  for (int j = 0; j < 4; ++j) d[0][j] = mfma32(K[j], qf, f32x4{0.f, 0.f, 0.f, 0.f});
#pragma unroll
  for (int u = 0; u < 4; ++u) {
    SCHED_FENCE();
    if (u < 3) {
#pragma unroll
      for (int j = 0; j < 4; ++j) d[(u + 1) & 1][j] = mfma32(K[4 * u + 4 + j], qf, f32x4{0.f, 0.f, 0.f, 0.f});
    }
#pragma unroll
    for (int j = 0; j < 4; ++j) {
      const f32x4 e = d[u & 1][j];
      sc[4 * u + j] = dpp<0xFF>(e[3]) + (dpp<0xAA>(e[2]) + (dpp<0x55>(e[1]) + dpp<0x00>(e[0])));
    }
  }
  SCHED_FENCE();
  float m = fmaxf(fmaxf(sc[0], sc[1]), sc[2]);
#pragma unroll
  for (int i = 3; i < 15; i += 2) m = fmaxf(fmaxf(m, sc[i]), sc[i + 1]);
  m = xrow_max(fmaxf(m, sc[15])) * C2;
  float l = 0.f;
#pragma unroll
  for (int i = 0; i < 16; ++i) { sc[i] = __builtin_amdgcn_exp2f(fmaf(sc[i], C2, -m)); l += sc[i]; }
  l = xrow_sum(l);
  if (lane < 16 && (lane & 3) == 0) { mx[hl * 8 + w] = m; ls[hl * 8 + w] = l; }
#pragma unroll
  for (int u = 0; u < 2; ++u)
#pragma unroll
    for (int t = 0; t < 4; ++t) pa[u][t] = cvt_pk_bf16(sc[8 * u + 2 * t], sc[8 * u + 2 * t + 1]);
}
__device__ __forceinline__ float wave_scale(const float* mx, const float* ls, int g, int w, float& lse) {
  const f32x4 m0 = *(const f32x4*)&mx[g * 8], m1 = *(const f32x4*)&mx[g * 8 + 4];
  const f32x4 l0 = *(const f32x4*)&ls[g * 8], l1 = *(const f32x4*)&ls[g * 8 + 4];
  const float mo = mx[g * 8 + w];
  const float M = fmaxf(fmaxf(fmaxf(m0[0], m0[1]), fmaxf(m0[2], m0[3])), fmaxf(fmaxf(m1[0], m1[1]), fmaxf(m1[2], m1[3])));
  float L = 0.f;
#pragma unroll
  for (int j = 0; j < 4; ++j) L += l0[j] * __builtin_amdgcn_exp2f(m0[j] - M) + l1[j] * __builtin_amdgcn_exp2f(m1[j] - M);
  lse = M + __builtin_amdgcn_logf(L);
  return __builtin_amdgcn_exp2f(mo - M) * __builtin_amdgcn_rcpf(L);
}

constexpr int sched(int n) {
  return (n <= 160 ? n * PACE_K : n <= 288 ? 160 * PACE_K + (n - 160) * PACE_V : 160 * PACE_K + 128 * PACE_V + (n - 288) * PACE_W) / 100 + PACE_C;
}

__global__ void __launch_bounds__(512, 2) fused_decode(Params p) {
  __shared__ __attribute__((aligned(16))) float lds[4 * 8 * 128 + 8 * 512 + 256 + 64 + 12 + 4 * 68 * 4];
  float* red = lds;
  float* redo = lds + 4 * 8 * 128;
  unsigned* q_s = (unsigned*)(redo + 8 * 512);
  float* mx = redo + 8 * 512 + 256;
  float* ls = mx + 32;
  unsigned* cnt = (unsigned*)(ls + 32);
  v4u* a_s = (v4u*)(cnt + 12);

  const int bid = blockIdx.x;
  v16u ka, kb;
  u64 tt0;
  asm volatile("s_load_dwordx16 %0, %3, %4\n\ts_load_dwordx16 %1, %3, %4 offset:64\n\ts_memrealtime %2"
               : "=&s"(ka), "=&s"(kb), "=&s"(tt0) : "s"(__builtin_amdgcn_kernarg_segment_ptr()), "s"(bid & 128));
  SCHED_FENCE();
  const int tid = threadIdx.x, lane = tid & 63, w = __builtin_amdgcn_readfirstlane(tid >> 6);
  unsigned cpf;
  {
    u64 pc;
    unsigned len;
    asm volatile("s_getpc_b64 %0\n.Lfd_pc:\n\ts_mov_b32 %1, .Lfd_end-.Lfd_pc" : "=s"(pc), "=s"(len));
    const unsigned x = tid ^ 256, co = ((bid >> 3) & (CPT / CPF - 1)) * CPF;
    cpf = bload32(v4u{(unsigned)pc, (unsigned)(pc >> 32), len, 0x00020000u}, co + ((x << 6) | ((x / (CPF / 64)) << 16)), 0, 0);
  }
  SCHED_FENCE();
  const int kvh = (bid >> 3) & 7, mem = (bid & 7) * 2 + ((bid >> 6) & 1);
  const int g = lane >> 4, c = lane & 15;
  const int b = mem >> 1, half = mem & 1, hq = mem >> 2, cq = (mem ^ kvh) & 3;
  const int jj = kvh * 4 + hq;
  const int w4 = w & 3;
  const int kvbase = ((b * 8 + kvh) * 4096 + 3072) * 256 + (half << B_H) + ((w & 1) << B_W0) + (((w >> 1) & 1) << B_W1) + ((w >> 2) << B_W2);
  int koff = kvbase + (((lane >> 2) & 1) << B_L0) + (((lane >> 3) & 1) << B_L1) + 64 * g + 16 * (lane & 3);
  const int voff = kvbase + ((g & 1) << B_L0) + ((g >> 1) << B_L1) + 16 * c;
  const int memq = mem ^ (kvh << 1);
  int qwoff = ((256 * half + 64 * w + 8 * (lane >> 3)) * 4096 + kvh * 512 + 64 * (memq >> 1) + 8 * (lane & 7)) * 2;
  int xoff = ((c & 7) * 512 + 256 * half + 64 * w + 16 * g + 8 * (c >> 3)) * 2;
  if (tid < 9) cnt[tid] = 0u;
  SCHED_FENCE();
  asm volatile("" : "+v"(koff), "+v"(qwoff), "+v"(xoff));
  asm volatile("s_waitcnt lgkmcnt(0)" : "+s"(ka), "+s"(kb), "+s"(tt0) :: "memory");
  SCHED_FENCE();
  const v4u rk = DESC(ka, D_K), rws = DESC(ka, D_WS);
  v4u K[16], V[16], pa[2], wq[8], xa;
#define rv DESC(kb, D_V - 4)
#define rwo DESC(kb, D_WO - 4)
  const unsigned lbase = (unsigned)(uintptr_t)(__attribute__((address_space(3))) float*)lds;
  f32x4 oa[8];
  unsigned t0 = (unsigned)tt0;

#define LOAD_K(i) K[i] = bload128(rk, koff, toff(i), AUX_NT)
#define LOAD_V(i) V[i] = bload128(rv, voff, toff(i), AUX_NT)
#define PACED(n) do { SCHED_FENCE(); while ((unsigned)__builtin_amdgcn_s_memrealtime() - t0 - stg < (unsigned)sched(n)) {} SCHED_FENCE(); } while (0)
#define TAGS(ep) const u64 T = (((u64)__builtin_amdgcn_readfirstlane(ep[1]) << 32) | (u64)__builtin_amdgcn_readfirstlane(ep[0])) + 1ull; \
                 const unsigned tagA = (unsigned)T, tagH = (unsigned)(T >> 32), tagB = tagH ^ tagA
#define CNT_OFF(i) ((4 * 8 * 128 + 8 * 512 + 256 + 64 + (i)) * 4)
#define ARRIVE_AT(a, i) asm volatile("s_mov_b64 exec, 1\n\tds_add_u32 %0, %1 offset:%2\n\ts_mov_b64 exec, -1" :: "v"(a), "v"(1u), "n"(CNT_OFF(i)) : "memory")
#define ARRIVE(i) ARRIVE_AT(lbase, i)
#define AWAIT(i, n) do { asm volatile("" ::: "memory"); \
    while (__builtin_amdgcn_readfirstlane(__hip_atomic_load(&cnt[i], __ATOMIC_RELAXED, __HIP_MEMORY_SCOPE_WORKGROUP)) < (n)) {} \
    asm volatile("" ::: "memory"); } while (0)
#define COMMON_START() do { const unsigned n_ = (unsigned)__builtin_amdgcn_readfirstlane(__hip_atomic_load(&cnt[C_S], __ATOMIC_RELAXED, __HIP_MEMORY_SCOPE_WORKGROUP)); \
    asm volatile("" ::: "memory"); \
    const unsigned s_ = (unsigned)__builtin_amdgcn_readfirstlane(__hip_atomic_load(&cnt[C_T], __ATOMIC_RELAXED, __HIP_MEMORY_SCOPE_WORKGROUP)); \
    const int d_ = (int)(s_ - (t0 << 4)) >> 4; \
    t0 += n_ < 4 ? 0u : (unsigned)min(max(d_, -150), 150); } while (0)
#define MERGE_O() do { \
    *(f32x4*)&redo[(w * 4 + g) * 128 + 8 * c] = f32x4{oa[0][0], oa[1][0], oa[2][0], oa[3][0]} * fsc; \
    *(f32x4*)&redo[(w * 4 + g) * 128 + 8 * c + 4] = f32x4{oa[4][0], oa[5][0], oa[6][0], oa[7][0]} * fsc; \
    ARRIVE(C_M); \
  } while (0)
#define R2(i) (*(const f32x2*)(r0 + 512 * (i)))
  enum { C_Q, C_S, C_Y, C_M, C_A, C_T = 8 };

  if (w < 4) {
    xa = bload128(DESC(ka, D_X), xoff, 0, 0);
    SCHED_FENCE();
#pragma unroll
    for (int i = 0; i < 8; ++i) wq[i] = bload128(DESC(ka, D_WQ), qwoff, i * 8192, AUX_NT);
    SCHED_FENCE();
  } else {
#pragma unroll
    for (int i = 0; i < 8; ++i) asm volatile("" : "=v"(wq[i]));
  }
  const v2u ep = bload64(rws, WS_EPOCH, 0, AUX_SC1);
  BARRIER_ONLY();
  SCHED_FENCE();
  {
    const unsigned t1 = (unsigned)__builtin_amdgcn_s_memrealtime();
    t0 += (unsigned)max((int)(t1 - t0) - RB, 0);
    SCHED_FENCE();
  }
  SCHED_FENCE();
  const unsigned stg = (unsigned)(w4 * STGW + (mem & 3) * STGC - (3 * STGW + 3 * STGC) / 2);
  int wr = w;
  asm volatile("" : "+s"(wr));
  if (__builtin_expect(wr < 4, 1)) {
    {
      f32x4 d[8];
#pragma unroll
      for (int dd = 0; dd < 8; ++dd) d[dd] = mfma32(xa, col_frag(wq, dd), f32x4{0.f, 0.f, 0.f, 0.f});
      if ((g >> 1) == (c >> 3)) {
#pragma unroll
        for (int r = 0; r < 4; ++r) {
          float* r0 = &red[((2 * w + (g >> 1)) * 8 + 4 * (g & 1) + r) * 64 + 8 * (c & 7)];
          *(f32x4*)(r0) = f32x4{d[0][r], d[1][r], d[2][r], d[3][r]};
          *(f32x4*)(r0 + 4) = f32x4{d[4][r], d[5][r], d[6][r], d[7][r]};
        }
      }
    }
    ARRIVE(C_Q);
    TAGS(ep);
    (void)tagH;
    AWAIT(C_Q, 4);
    {
      const float* r0 = &red[2 * tid];
      const f32x2 s = ((R2(0) + R2(1)) + (R2(2) + R2(3))) + ((R2(4) + R2(5)) + (R2(6) + R2(7)));
      bstore128(v4u{__float_as_uint(s[0]), tagA, __float_as_uint(s[1]), tagB}, rws,
                WS_QG + ((kvh * 8 + (tid >> 5)) * 16 + memq) * 512 + (lane & 31) * 16, 0, AUX_SC1);
      if (tid == 0) bstore128(v4u{t0, tagA, t0, tagB}, rws, WS_TG + (kvh * 16 + mem) * 16, 0, AUX_SC1);
    }
    SCHED_FENCE();
    asm volatile("" :: "v"(cpf));
    SCHED_FENCE();
#pragma unroll
    for (int i = 0; i < 16; ++i) { PACED(96 + 4 * i); LOAD_K(i); }
    SCHED_FENCE();
    COMMON_START();
#pragma unroll
    for (int i = 0; i < 16; ++i) { PACED(160 + 4 * i); LOAD_V(i); }
    SCHED_FENCE();
    AWAIT(C_S, 4);
    scores_softmax(K, q_s, mx, ls, lane, w, pa);
    ARRIVE(C_Y);
    SCHED_FENCE();
    AWAIT(C_Y, 8);
    {
      float lse;
      const float fsc = wave_scale(mx, ls, g, w, lse);
      if (lane == 16 * w)
        bstore128(v4u{__float_as_uint(lse), tagA, __float_as_uint(lse), tagB}, rws, WS_LG + ((kvh * 4 + w) * 16 + mem) * 16, 0, AUX_SC1);
#pragma unroll
      for (int dd = 0; dd < 8; ++dd) oa[dd] = mfma32(pa[0], col_frag(V, dd), f32x4{0.f, 0.f, 0.f, 0.f});
#pragma unroll
      for (int dd = 0; dd < 8; ++dd) oa[dd] = mfma32(pa[1], col_frag(V + 8, dd), oa[dd]);
      MERGE_O();
    }
    SCHED_FENCE();
    COMMON_START();
    v4u wo[8];
    {
      const int off = ((jj * 128 + 32 * w + 4 * g) * 512 + 128 * cq + 8 * c) * 2;
#pragma unroll
      for (int i = 0; i < 8; ++i) { PACED(288 + 4 * i); wo[i] = bload128(rwo, off + (i & 3) * 1024, (i >> 2) * 16384, AUX_NT); }
    }
    SCHED_FENCE();
    {
      const int loff = WS_LG + ((kvh * 4 + hq) * 16 + 4 * g + (c & 3)) * 16;
      v4u e = bload128(rws, loff, 0, AUX_SC1);
      SCHED_FENCE();
      v4u wb[8];
#pragma unroll
      for (int dd = 0; dd < 8; ++dd) { wb[dd] = col_frag(wo, dd); asm volatile("" : "+v"(wb[dd])); }
      SCHED_FENCE();
      unsigned spins = 0;
#pragma nounroll
      for (;;) {
        const bool be = ((e[1] ^ tagA) | (e[3] ^ tagB)) != 0u;
        if (!__any(be) || ++spins >= SPIN_CAP) break;
        asm volatile("" ::: "memory");
        if (be) e = bload128(rws, loff, 0, AUX_SC1);
      }
      const float eo = __uint_as_float(e[0]);
      const float wt = __builtin_amdgcn_rcpf(1.f + __builtin_amdgcn_exp2f(dpp<0xB1>(eo) - eo));
      float w0 = dpp<0x00>(wt), w1 = dpp<0x55>(wt), w2 = dpp<0xAA>(wt), w3 = dpp<0xFF>(wt);
      asm volatile("" : "+v"(w0), "+v"(w1), "+v"(w2), "+v"(w3));
      SCHED_FENCE();
      AWAIT(C_A + w, 1);
      const v4u af = a_s[w * 68 + g * 17 + c];
      f32x4 d[8];
#pragma unroll
      for (int dd = 0; dd < 8; ++dd) d[dd] = mfma32(af, wb[dd], f32x4{0.f, 0.f, 0.f, 0.f});
      float* r0 = &red[(w * 8 + 2 * g) * 128 + 8 * c];
#pragma unroll
      for (int j = 0; j < 2; ++j) {
        f32x4 y0, y1;
#pragma unroll
        for (int dd = 0; dd < 4; ++dd) {
          y0[dd] = fmaf(w0, d[4 * j + dd][0], w1 * d[4 * j + dd][1]);
          y1[dd] = fmaf(w2, d[4 * j + dd][2], w3 * d[4 * j + dd][3]);
        }
        *(f32x4*)(r0 + 4 * j) = y0;
        *(f32x4*)(r0 + 128 + 4 * j) = y1;
      }
    }
    WG_BARRIER();
    {
      const float* r0 = &red[2 * w * 128 + 4 * lane];
      const f32x4 s = (*(const f32x4*)r0 + *(const f32x4*)(r0 + 1024)) + (*(const f32x4*)(r0 + 2048) + *(const f32x4*)(r0 + 3072));
      bstore128(v4u{cvt_pk_bf16(s[0], s[1]), tagA, cvt_pk_bf16(s[2], s[3]), tagB}, rws,
                WS_PG + ((cq * 32 + 8 * w + (lane >> 3)) * 32 + jj) * 128 + (lane & 7) * 16, 0, AUX_SC1);
    }
    return;
  }

  LOAD_K(0);
#pragma unroll
  for (int i = 1; i < 16; ++i) { PACED(32 + 4 * i); LOAD_K(i); }
  SCHED_FENCE();
  TAGS(ep);
  {
    const int qoff = WS_QG + ((kvh * 8 + b) * 4 + w4) * 2048 + lane * 16;
    const int tgoff = WS_TG + (kvh * 16 + 4 * w4 + (lane & 3)) * 16;
    v4u q0 = bload128(rws, qoff, 0, AUX_SC1);
    v4u q1 = bload128(rws, qoff + 1024, 0, AUX_SC1);
    v4u tq = {t0, tagA, t0, tagB};
    if (lane < 4) tq = bload128(rws, tgoff, 0, AUX_SC1);
    unsigned spins = 0;
#pragma nounroll
    for (;;) {
      const bool ba = ((q0[1] ^ tagA) | (q0[3] ^ tagB)) != 0u;
      const bool bb = ((q1[1] ^ tagA) | (q1[3] ^ tagB)) != 0u;
      const bool bt = ((tq[1] ^ tagA) | (tq[3] ^ tagB)) != 0u;
      if (!__any(ba || bb || bt) || ++spins >= SPIN_CAP) break;
      asm volatile("" ::: "memory");
      if (ba) q0 = bload128(rws, qoff, 0, AUX_SC1);
      if (bb) q1 = bload128(rws, qoff + 1024, 0, AUX_SC1);
      if (bt) tq = bload128(rws, tgoff, 0, AUX_SC1);
    }
    asm volatile("" :: "v"(cpf));
    const auto u0 = __builtin_amdgcn_permlane32_swap(q0[0], q1[0], false, false);
    const auto u1 = __builtin_amdgcn_permlane32_swap(q0[2], q1[2], false, false);
    q_s[w4 * 64 + lane] = cvt_pk_bf16(__uint_as_float(u0[0]) + __uint_as_float(u0[1]), __uint_as_float(u1[0]) + __uint_as_float(u1[1]));
    asm volatile("s_mov_b64 exec, 15\n\tds_add_u32 %0, %1 offset:%2\n\ts_mov_b64 exec, -1" :: "v"(lbase), "v"(tq[0]), "n"(CNT_OFF(C_T)) : "memory");
  }
  ARRIVE(C_S);
  AWAIT(C_S, 4);
  COMMON_START();
  scores_softmax(K, q_s, mx, ls, lane, w, pa);
  ARRIVE(C_Y);
  SCHED_FENCE();
#pragma unroll
  for (int i = 0; i < 16; ++i) { PACED(224 + 4 * i); LOAD_V(i); }
  SCHED_FENCE();
  AWAIT(C_Y, 8);
  {
    float lse;
    float fsc = wave_scale(mx, ls, g, w, lse);
    asm volatile("" : "+v"(fsc));
    SCHED_FENCE();
#pragma unroll
    for (int dd = 0; dd < 8; ++dd) oa[dd] = mfma32(pa[0], col_frag(V, dd), f32x4{0.f, 0.f, 0.f, 0.f});
    SCHED_FENCE();
#pragma unroll
    for (int dd = 0; dd < 8; ++dd) oa[dd] = mfma32(pa[1], col_frag(V + 8, dd), oa[dd]);
    MERGE_O();
  }
  AWAIT(C_M, 8);
  {
    const float* r0 = &redo[2 * (tid - 256)];
    const f32x2 s = ((R2(0) + R2(1)) + (R2(2) + R2(3))) + ((R2(4) + R2(5)) + (R2(6) + R2(7)));
    const unsigned p01 = cvt_pk_bf16(s[0], s[1]);
    const unsigned p23 = (unsigned)__builtin_amdgcn_mov_dpp((int)p01, 0xF5, 0xf, 0xf, true);
    if (!(lane & 1))
      bstore128(v4u{p01, tagA, p23, tagB}, rws, WS_OG + ((kvh * 4 + w4) * 16 + mem) * 512 + (lane >> 1) * 16, 0, AUX_SC1);
  }
  {
    const int ooff = WS_OG + ((kvh * 4 + hq) * 16 + (lane >> 2)) * 512 + w4 * 128 + (lane & 3) * 16;
    __builtin_amdgcn_s_sleep(ODELAY);
    v4u o0 = bload128(rws, ooff, 0, AUX_SC1);
    v4u o1 = bload128(rws, ooff + 64, 0, AUX_SC1);
    unsigned spins = 0;
#pragma nounroll
    for (;;) {
      const bool bo = (((o0[1] ^ tagA) | (o0[3] ^ tagB)) | ((o1[1] ^ tagA) | (o1[3] ^ tagB))) != 0u;
      if (!__any(bo) || ++spins >= SPIN_CAP) break;
      asm volatile("" ::: "memory");
      if (bo) {
        o0 = bload128(rws, ooff, 0, AUX_SC1);
        o1 = bload128(rws, ooff + 64, 0, AUX_SC1);
      }
    }
    a_s[w4 * 68 + (lane & 3) * 17 + (lane >> 2)] = v4u{o0[0], o0[2], o1[0], o1[2]};
    ARRIVE_AT(lbase + 4 * w4, C_A);
  }
  if (w4 >= 2) { BARRIER_ONLY(); return; }
  const int poff = WS_PG + (cq * 32 + jj) * 4096 + (lane >> 2) * 128 + w4 * 64 + (lane & 3) * 16;
  BARRIER_ONLY();
  __builtin_amdgcn_s_sleep(PDELAY);
  v4u pg0 = bload128(rws, poff, 0, AUX_SC1);
  v4u pg1 = bload128(rws, poff + 2048, 0, AUX_SC1);
  bool b0 = ((pg0[1] ^ tagA) | (pg0[3] ^ tagB)) != 0u, b1 = ((pg1[1] ^ tagA) | (pg1[3] ^ tagB)) != 0u;
  unsigned spins = 0;
#pragma nounroll
  while (__any(b0 || b1) && ++spins < SPIN_CAP) {
    asm volatile("" ::: "memory");
    if (b0) pg0 = bload128(rws, poff, 0, AUX_SC1);
    if (b1) pg1 = bload128(rws, poff + 2048, 0, AUX_SC1);
    b0 = ((pg0[1] ^ tagA) | (pg0[3] ^ tagB)) != 0u;
    b1 = ((pg1[1] ^ tagA) | (pg1[3] ^ tagB)) != 0u;
  }
  float sv[4] = {__uint_as_float(pg0[0] << 16) + __uint_as_float(pg1[0] << 16), __uint_as_float(pg0[0] & 0xffff0000u) + __uint_as_float(pg1[0] & 0xffff0000u),
                 __uint_as_float(pg0[2] << 16) + __uint_as_float(pg1[2] << 16), __uint_as_float(pg0[2] & 0xffff0000u) + __uint_as_float(pg1[2] & 0xffff0000u)};
#pragma unroll
  for (int i = 0; i < 4; ++i) { sv[i] += dpp<0x124>(sv[i]); sv[i] += dpp<0x128>(sv[i]); sv[i] = xrow_sum(sv[i]); }
  if (lane < 4) bstore64(v2u{cvt_pk_bf16(sv[0], sv[1]), cvt_pk_bf16(sv[2], sv[3])}, DESC(kb, D_OUT - 4), ((jj >> 2) * 1024 + 128 * cq + 32 * (jj & 3) + 16 * w4) * 2 + lane * 8, 0, AUX_SC1);
  if (jj == 0 && cq == 0 && tid == 256)
    bstore64(v2u{tagA, tagH}, rws, WS_EPOCH, 0, AUX_SC1);
  asm volatile(".Lfd_end:");
}

extern "C" void kernel_launch(void* const* d_in, const int* in_sizes, int n_in, void* d_out, int out_size, void* d_ws, size_t ws_size,
                              hipStream_t stream) {
  constexpr unsigned FMT = 0x00020000u;
  Params p;
  for (int br = 0; br < 2; ++br) {
    void* const* in = d_in + 5 * br;
    p.d[br][D_X] = Desc{in[0], 8 * 512 * 2, FMT};
    p.d[br][D_K] = Desc{in[1], 8 * 8 * 4096 * 128 * 2, FMT};
    p.d[br][D_V] = Desc{in[2], 8 * 8 * 4096 * 128 * 2, FMT};
    p.d[br][D_WQ] = Desc{in[3], 512 * 4096 * 2, FMT};
    p.d[br][D_WO] = Desc{in[4], 512 * 4096 * 2, FMT};
    p.d[br][D_WS] = Desc{(char*)d_ws + br * WS_BRANCH, WS_END, FMT};
    p.d[br][D_OUT] = Desc{(char*)d_out + br * 1024, 16384u - br * 1024, FMT};
    p.d[br][D_NONE] = Desc{nullptr, 0, FMT};
  }
  hipLaunchKernelGGL(fused_decode, dim3(256), dim3(512), 0, stream, p);
}
```
